```python
import torch

def _softmax(x, axis=-1):
    return torch.softmax(x, dim=axis)
def _gelu(x):
    return torch.nn.functional.gelu(x, approximate="tanh")
def _silu(x):
    return torch.nn.functional.silu(x)
def _sigmoid(x):
    return torch.sigmoid(x)
def _layernorm(x, axis=-1, eps=1e-5):
    m = x.mean(dim=axis, keepdim=True)
    v = x.var(dim=axis, keepdim=True, unbiased=False)
    return (x - m) / torch.sqrt(v + eps)
def _scatter_add(tgt, idx, src, axis=0):
    return tgt.clone().index_add_(axis, idx, src)
def _segment_sum(src, seg, n):
    out = torch.zeros((n,) + src.shape[1:], dtype=src.dtype)
    return out.index_add_(0, seg, src)
def _pad(x, pw):
    flat = tuple(v for pair in reversed(pw) for v in pair)
    return torch.nn.functional.pad(x, flat)

kernel_name = 'parallel_sum__gqa_block_cached__glu_gated_bilinear__decode'

def setup_inputs():

    _w = (lambda a: (lambda t: t.to(torch.bfloat16) if t.is_floating_point() else t)(torch.as_tensor(a)))
    def _A_setup():
        B, Sq, D = 2, 1, 576
        Hq, Hkv, Sk, Dh = 32, 16, 4096, 128
        assert Hq % Hkv == 0, (Hq, Hkv)
        return {
            "x": _w((lambda *s: torch.randn(*s, dtype=torch.bfloat16) * 0.05)(B, Sq, D)),
            "k_cache": _w((lambda *s: torch.randn(*s, dtype=torch.bfloat16) * 1.6)(B, Hkv, Sk, Dh)),
            "v_cache": _w((lambda *s: torch.randn(*s, dtype=torch.bfloat16) * 0.8)(B, Hkv, Sk, Dh)),
            "wq": _w((lambda *s: torch.randn(*s, dtype=torch.bfloat16) * 1.6)(D, Hq * Dh) * (D ** -0.5)),
            "wo": _w((lambda *s: torch.randn(*s, dtype=torch.bfloat16) * 0.8)(Hq * Dh, D) * ((Hq * Dh) ** -0.5)),
        }
    _A_in = _A_setup()

    def _B_setup():
        B, S, D, F = 2, 1, 576, 4608
        return {
            "x": _w((lambda *s: torch.randn(*s, dtype=torch.bfloat16) * 0.05)(B, S, D)),
            "w_gate": _w((lambda *s: torch.randn(*s, dtype=torch.bfloat16) * 0.4)(D, F) * (D ** -0.5)),
            "w_up": _w((lambda *s: torch.randn(*s, dtype=torch.bfloat16) * 0.4)(D, F) * (D ** -0.5)),
            "w_down": _w((lambda *s: torch.randn(*s, dtype=torch.bfloat16) * 0.4)(F, D) * (F ** -0.5)),
        }
    _B_in = _B_setup()

    return {
        "A_x": _A_in["x"],
        "A_k_cache": _A_in["k_cache"],
        "A_v_cache": _A_in["v_cache"],
        "A_wq": _A_in["wq"],
        "A_wo": _A_in["wo"],
        "B_x": _B_in["x"],
        "B_w_gate": _B_in["w_gate"],
        "B_w_up": _B_in["w_up"],
        "B_w_down": _B_in["w_down"],
    }

def reference(A_x, A_k_cache, A_v_cache, A_wq, A_wo, B_x, B_w_gate, B_w_up, B_w_down):
    def _A_ref(x, k_cache, v_cache, wq, wo):
        B, Sq, D = x.shape
        Hq, Dh = 32, 128
        Hkv = k_cache.shape[1]
        rep = Hq // Hkv
        q = (lambda x, *d: x.permute(*d))((x @ wq).reshape(B, Sq, Hq, Dh), 0, 2, 1, 3)
        k = (lambda x, n, axis: torch.repeat_interleave(x, n, dim=axis))(k_cache, rep, axis=1)
        v = (lambda x, n, axis: torch.repeat_interleave(x, n, dim=axis))(v_cache, rep, axis=1)
        scores = torch.einsum("bhqd,bhkd->bhqk", q, k) * (Dh ** -0.5)
        p = _softmax(scores, axis=-1)
        o = torch.einsum("bhqk,bhkd->bhqd", p, v)
        return (lambda x, *d: x.permute(*d))(o, 0, 2, 1, 3).reshape(B, Sq, Hq * Dh) @ wo

    def _B_ref(x, w_gate, w_up, w_down):
        g = torch.einsum("bsd,df->bsf", x, w_gate)
        u = torch.einsum("bsd,df->bsf", x, w_up)
        h = g * u
        return torch.einsum("bsf,fd->bsd", h, w_down)

    x = A_x
    xn = _layernorm(x, axis=-1)
    return x + _A_ref(xn, A_k_cache, A_v_cache, A_wq, A_wo) + _B_ref(xn, B_w_gate, B_w_up, B_w_down)
```

```cpp
#include <hip/hip_runtime.h>
#include <hip/hip_bf16.h>
#include <stdint.h>

typedef unsigned short u16;
typedef unsigned int u32;
using u32x4 = __attribute__((ext_vector_type(4))) unsigned;

constexpr int D = 576, HQ = 32, HKV = 16, SK = 4096, DH = 128, F = 4608, NB = 2;
#define MIMIC 0

__device__ __forceinline__ float bf2f(u16 x) { return __uint_as_float(((u32)x) << 16); }
__device__ __forceinline__ float rbf(float x) {
  __bf16 b = (__bf16)x; return (float)b;
}
__device__ __forceinline__ u16 f2bf(float x) { __bf16 b = (__bf16)x; return __builtin_bit_cast(u16, b); }

__device__ __forceinline__ void layernorm(const u16* x, float* xn  , float* red) {
  const int tid = threadIdx.x;
  for (int b = 0; b < NB; ++b) {
    float s = 0.f;
    for (int i = tid; i < D; i += 256) s += bf2f(x[b * D + i]);
    for (int o = 32; o > 0; o >>= 1) s += __shfl_xor(s, o);
    if ((tid & 63) == 0) red[tid >> 6] = s;
    __syncthreads();
    float mf = (red[0] + red[1] + red[2] + red[3]) * (1.f / D);
    __syncthreads();
    float v = 0.f;
    for (int i = tid; i < D; i += 256) { float d = bf2f(x[b * D + i]) - mf; v += d * d; }
    for (int o = 32; o > 0; o >>= 1) v += __shfl_xor(v, o);
    if ((tid & 63) == 0) red[tid >> 6] = v;
    __syncthreads();
    float vf = (red[0] + red[1] + red[2] + red[3]) * (1.f / D);
    __syncthreads();
    float m = rbf(mf), var = rbf(vf);
    float den = rbf(sqrtf(rbf(var + 1e-5f)));
    for (int i = tid; i < D; i += 256) xn[b * D + i] = rbf(rbf(bf2f(x[b * D + i]) - m) / den);
  }
  __syncthreads();
}

__global__ void __launch_bounds__(256) k_first(const u16* x, const u16* wq, const u16* wg, const u16* wu, float* q, float* h) {
  __shared__ float xn[NB * D]; __shared__ float red[4]; __shared__ float part[4][4][64];
  layernorm(x, xn, red);
  const int tid = threadIdx.x, c = tid & 63, rg = tid >> 6;
  if (blockIdx.x < 64) {
    int col = blockIdx.x * 64 + c; float a0 = 0, a1 = 0;
    for (int r = rg; r < D; r += 4) { float w = bf2f(wq[(size_t)r * 4096 + col]); a0 += w * xn[r]; a1 += w * xn[D + r]; }
    part[rg][0][c] = a0; part[rg][1][c] = a1; __syncthreads();
    if (tid < 128) { int b = tid >> 6; float s = part[0][b][c] + part[1][b][c] + part[2][b][c] + part[3][b][c];
      q[b * 4096 + col] = rbf(s); }
  } else {
    int col = (blockIdx.x - 64) * 64 + c; float a0 = 0, a1 = 0, a2 = 0, a3 = 0;
    for (int r = rg; r < D; r += 4) { float w = bf2f(wg[(size_t)r * F + col]), w2 = bf2f(wu[(size_t)r * F + col]);
      a0 += w * xn[r]; a1 += w * xn[D + r]; a2 += w2 * xn[r]; a3 += w2 * xn[D + r]; }
    part[rg][0][c] = a0; part[rg][1][c] = a1; part[rg][2][c] = a2; part[rg][3][c] = a3; __syncthreads();
    if (tid < 128) { int b = tid >> 6;
      float g = part[0][b][c] + part[1][b][c] + part[2][b][c] + part[3][b][c];
      float u = part[0][2 + b][c] + part[1][2 + b][c] + part[2][2 + b][c] + part[3][2 + b][c];
      h[b * F + col] = rbf(rbf(g) * rbf(u)); }
  }
}

__global__ void __launch_bounds__(256) k_attn(const u16* K, const u16* V, const float* q, float* pm, float* po) {
  __shared__ float sc[2][512]; __shared__ float qs[2][128]; __shared__ float red[8]; __shared__ float oacc[4][2][128];
  const int tid = threadIdx.x, s = blockIdx.x & 7, bh = blockIdx.x >> 3, b = bh >> 4, kvh = bh & 15;
  qs[tid >> 7][tid & 127] = q[b * 4096 + kvh * 256 + tid];
  __syncthreads();
  const u16* Kb = K + ((size_t)bh * SK + s * 512) * DH; const u16* Vb = V + ((size_t)bh * SK + s * 512) * DH;
  const int c = tid & 15, r = tid >> 4;
  for (int k = r; k < 512; k += 16) {
    u32x4 kv = *(const u32x4*)(Kb + k * DH + c * 8); float a0 = 0, a1 = 0;
    for (int j = 0; j < 4; ++j) { float lo = __uint_as_float(kv[j] << 16), hi = __uint_as_float(kv[j] & 0xffff0000u);
      a0 += lo * qs[0][c * 8 + 2 * j] + hi * qs[0][c * 8 + 2 * j + 1]; a1 += lo * qs[1][c * 8 + 2 * j] + hi * qs[1][c * 8 + 2 * j + 1]; }
    for (int o = 8; o > 0; o >>= 1) { a0 += __shfl_xor(a0, o); a1 += __shfl_xor(a1, o); }
    if (c == 0) {
      sc[0][k] = a0 * 0.08838834764831845f; sc[1][k] = a1 * 0.08838834764831845f;
    }
  }
  __syncthreads();
  for (int g = 0; g < 2; ++g) {
    float m = fmaxf(sc[g][tid], sc[g][tid + 256]);
    for (int o = 32; o > 0; o >>= 1) m = fmaxf(m, __shfl_xor(m, o));
    if ((tid & 63) == 0) red[tid >> 6] = m; __syncthreads();
    m = fmaxf(fmaxf(red[0], red[1]), fmaxf(red[2], red[3])); __syncthreads();
    float p0 = __expf(sc[g][tid] - m), p1 = __expf(sc[g][tid + 256] - m); sc[g][tid] = p0; sc[g][tid + 256] = p1;
    float l = p0 + p1; for (int o = 32; o > 0; o >>= 1) l += __shfl_xor(l, o);
    if ((tid & 63) == 0) red[tid >> 6] = l; __syncthreads();
    l = red[0] + red[1] + red[2] + red[3]; __syncthreads();
    if (tid == 0) { pm[(blockIdx.x * 2 + g) * 2] = m; pm[(blockIdx.x * 2 + g) * 2 + 1] = l; }
  }
  float acc[2][8]; for (int j = 0; j < 8; ++j) { acc[0][j] = 0; acc[1][j] = 0; }
  for (int k = r; k < 512; k += 16) {
    u32x4 vv = *(const u32x4*)(Vb + k * DH + c * 8); float p0 = sc[0][k], p1 = sc[1][k];
    for (int j = 0; j < 4; ++j) { float lo = __uint_as_float(vv[j] << 16), hi = __uint_as_float(vv[j] & 0xffff0000u);
      acc[0][2 * j] += p0 * lo; acc[0][2 * j + 1] += p0 * hi; acc[1][2 * j] += p1 * lo; acc[1][2 * j + 1] += p1 * hi; }
  }
  for (int g = 0; g < 2; ++g) for (int j = 0; j < 8; ++j) { float a = acc[g][j]; a += __shfl_xor(a, 16); a += __shfl_xor(a, 32); if ((tid & 63) < 16) oacc[tid >> 6][g][c * 8 + j] = a; }
  __syncthreads();
  { int g = tid >> 7, d = tid & 127; po[(blockIdx.x * 2 + g) * 128 + d] = oacc[0][g][d] + oacc[1][g][d] + oacc[2][g][d] + oacc[3][g][d]; }
}

__global__ void __launch_bounds__(256) k_second(const u16* wo, const u16* wd, const float* pm, const float* po, const float* h, float* part) {
  __shared__ float in[2][64];
  const int tid = threadIdx.x, blk = blockIdx.x;
  const u16* W;
  if (blk < 64) {
    if (tid < 128) { int b = tid >> 6, row = blk * 64 + (tid & 63), head = row >> 7, d = row & 127, kvh = head >> 1, g = head & 1;
      int base = (b * 16 + kvh) * 8; float M = -INFINITY;
      for (int s = 0; s < 8; ++s) M = fmaxf(M, pm[((base + s) * 2 + g) * 2]);
      float L = 0, o = 0; for (int s = 0; s < 8; ++s) { float w = __expf(pm[((base + s) * 2 + g) * 2] - M); L += w * pm[((base + s) * 2 + g) * 2 + 1]; o += w * po[((base + s) * 2 + g) * 128 + d]; }
      in[b][tid & 63] = rbf(o / L); }
    W = wo + (size_t)blk * 64 * D;
  } else { if (tid < 128) in[tid >> 6][tid & 63] = h[(tid >> 6) * F + (blk - 64) * 64 + (tid & 63)]; W = wd + (size_t)(blk - 64) * 64 * D; }
  __syncthreads();
  for (int c = tid; c < D; c += 256) { float a0 = 0, a1 = 0; for (int r = 0; r < 64; ++r) { float w = bf2f(W[r * D + c]); a0 += w * in[0][r]; a1 += w * in[1][r]; }
    part[(blk * 2 + 0) * D + c] = a0; part[(blk * 2 + 1) * D + c] = a1; }
}
__global__ void __launch_bounds__(64) k_final(const u16* x, const float* part, u16* out) {
  int i = blockIdx.x * 64 + threadIdx.x; int b = i / D, c = i % D; float a = 0, f = 0;
  for (int k = 0; k < 64; ++k) a += part[(k * 2 + b) * D + c];
  for (int k = 64; k < 136; ++k) f += part[(k * 2 + b) * D + c];
  out[i] = f2bf(rbf(bf2f(x[i]) + rbf(a)) + rbf(f));
}

extern "C" void kernel_launch(void* const* d_in, const int* in_sizes, int n_in, void* d_out, int out_size, void* d_ws, size_t ws_size, hipStream_t stream) {
  const u16* x = (const u16*)d_in[0]; const u16* K = (const u16*)d_in[1]; const u16* V = (const u16*)d_in[2];
  const u16* wq = (const u16*)d_in[3]; const u16* wo = (const u16*)d_in[4];
  const u16* wg = (const u16*)d_in[6]; const u16* wu = (const u16*)d_in[7]; const u16* wd = (const u16*)d_in[8];
  float* ws = (float*)d_ws; float* q = ws; float* h = ws + 8192; float* pm = ws + 32768; float* po = ws + 65536; float* part = ws + 262144;
  k_first<<<136, 256, 0, stream>>>(x, wq, wg, wu, q, h);
  k_attn<<<256, 256, 0, stream>>>(K, V, q, pm, po);
  k_second<<<136, 256, 0, stream>>>(wo, wd, pm, po, h, part);
  k_final<<<18, 64, 0, stream>>>(x, part, (u16*)d_out);
}
```

```cpp
#include <hip/hip_runtime.h>
#include <hip/hip_bf16.h>
#include <stdint.h>

typedef unsigned short u16;
typedef unsigned int u32;
typedef unsigned long long u64;
using u32x4 = __attribute__((ext_vector_type(4))) unsigned;
using u32x3 = __attribute__((ext_vector_type(3))) unsigned;
using f32x4 = __attribute__((ext_vector_type(4))) float;
using f32x2 = __attribute__((ext_vector_type(2))) float;
using bf16x8 = __attribute__((ext_vector_type(8))) __bf16;
using bf16x2 = __attribute__((ext_vector_type(2))) __bf16;
typedef __attribute__((address_space(1))) const u32x4 gvec;
typedef __attribute__((address_space(1))) const char gchar;

constexpr int DM = 576, SK = 4096, FF = 4608;
constexpr u32 WS_E = 0;
constexpr u32 WS_ML = 4096;
constexpr u32 WS_PO = 65536;
constexpr u32 WS_P1 = WS_PO + 524288;
constexpr u32 WS_PF = WS_P1 + 8 * 13 * 4 * 2048;
constexpr u32 WS_END = WS_PF + 256 * 9216;
constexpr u32 SPIN_CAP = 1u << 20;

constexpr int L_XN = 0;
constexpr int L_Q = 1168;
constexpr int L_H = 1296;
constexpr int L_O = 1360;
constexpr int L_M = 1392;
constexpr int L_L = 1424;
constexpr int L_FIN = 1456;
constexpr int L_REC = 1600;
constexpr int L_ST = L_REC + 17 * 256;
constexpr int L_TOTAL = L_ST;
#define STAMP(id)
#define STAMP_INIT()
#define STAMP_DUMP()
#define FSLEEP 4
#define PSLEEP 4
#define ODELAY 12
#define FDELAY 10
#define UPROT 4
#define WMID 2

__device__ __forceinline__ float bf2f(u32 x) { return __uint_as_float(x << 16); }
__device__ __forceinline__ float rbf(float x) { return (float)(__bf16)x; }
__device__ __forceinline__ u32 pkbf(float lo, float hi) { bf16x2 v = {(__bf16)lo, (__bf16)hi}; return __builtin_bit_cast(u32, v); }
__device__ __forceinline__ u32x4 ldnt(gchar* p) { return __builtin_nontemporal_load((gvec*)p); }
__device__ __forceinline__ float dot2(u32 a, u32 b, float c) {
  return __builtin_amdgcn_fdot2_f32_bf16(__builtin_bit_cast(bf16x2, a), __builtin_bit_cast(bf16x2, b), c, false);
}
template <int CTRL> __device__ __forceinline__ float dpp(float x) {
  return __builtin_bit_cast(float, __builtin_amdgcn_mov_dpp(__builtin_bit_cast(int, x), CTRL, 0xf, 0xf, true));
}
constexpr int XOR1 = 0xB1, XOR2 = 0x4E, XOR7 = 0x141, ROR8 = 0x128, ROR4 = 0x124;
__device__ __forceinline__ float x16sum(float x) {
  auto s = __builtin_amdgcn_permlane16_swap(__float_as_uint(x), __float_as_uint(x), false, false);
  return __uint_as_float(s[0]) + __uint_as_float(s[1]);
}
__device__ __forceinline__ float x32sum(float x) {
  auto s = __builtin_amdgcn_permlane32_swap(__float_as_uint(x), __float_as_uint(x), false, false);
  return __uint_as_float(s[0]) + __uint_as_float(s[1]);
}
__device__ __forceinline__ float x16max(float x) {
  auto s = __builtin_amdgcn_permlane16_swap(__float_as_uint(x), __float_as_uint(x), false, false);
  return fmaxf(__uint_as_float(s[0]), __uint_as_float(s[1]));
}
__device__ __forceinline__ float x32max(float x) {
  auto s = __builtin_amdgcn_permlane32_swap(__float_as_uint(x), __float_as_uint(x), false, false);
  return fmaxf(__uint_as_float(s[0]), __uint_as_float(s[1]));
}
__device__ __forceinline__ float wavesum(float s) {
  s += dpp<XOR1>(s); s += dpp<XOR2>(s); s += dpp<XOR7>(s); s += dpp<ROR8>(s); return x32sum(x16sum(s));
}
__device__ __forceinline__ f32x4 rowmma(u32x4 a, u32 bb, u32x4 mk, f32x4 acc) {
  u32x4 b = {bb & mk[0], bb & mk[1], bb & mk[2], bb & mk[3]};
  return __builtin_amdgcn_mfma_f32_16x16x32_bf16(__builtin_bit_cast(bf16x8, a), __builtin_bit_cast(bf16x8, b), acc, 0, 0, 0);
}
#define CLOBBER() asm volatile("" ::: "memory")
#define BAR() do { asm volatile("s_waitcnt lgkmcnt(0)" ::: "memory"); __builtin_amdgcn_s_barrier(); asm volatile("" ::: "memory"); } while (0)
#define VMW_(n) asm volatile("s_waitcnt vmcnt(" #n ")" ::: "memory")
#define VMW(n) VMW_(n)
#define RETRY(LOADS, OK, SL) do { if (!__all(OK)) { _Pragma("unroll 1") for (u32 spins = 0; spins < SPIN_CAP; ++spins) { \
      __builtin_amdgcn_s_sleep(SL); CLOBBER(); if (!(OK)) { LOADS; } if (__all(OK)) break; } } } while (0)
#define RETRY_ALL(LOADS, OK, SL) do { if (!__all(OK)) { _Pragma("unroll 1") for (u32 spins = 0; spins < SPIN_CAP; ++spins) { \
      __builtin_amdgcn_s_sleep(SL); CLOBBER(); LOADS; if (__all(OK)) break; } } } while (0)
__device__ __forceinline__ void gstore(__amdgpu_buffer_rsrc_t r, u32 off, float a, float b, u32 T) {
  u32x4 v = {__float_as_uint(a), T, __float_as_uint(b), T};
  __builtin_amdgcn_raw_buffer_store_b128(v, r, off, 0, 16);
}
__device__ __forceinline__ u32x4 gload(__amdgpu_buffer_rsrc_t r, u32 off) { return __builtin_amdgcn_raw_buffer_load_b128(r, off, 0, 16); }
__device__ __forceinline__ bool gok(u32x4 v, u32 T) { return v[1] == T && v[3] == T; }

struct Ctx {
  const u16* X; const u16* Wo; const u16* Wd; u16* out; char* ws; gchar* kp; gchar* vp; u32* lds;
  __amdgpu_buffer_rsrc_t rs; u32x4 mk; u32 T;
  int lane, wv, w, s0, cnt, bA, sB;
};
enum { R_RED, R_QG, R_HG, R_ML, R_PLAIN, R_L2, R_L2H };

template <int ROLE>
__device__ __forceinline__ void rest(const Ctx& cx, u32x4 k0, u32x4 k1, u32x4 k2) {
  u32* lds = cx.lds; float* ldf = reinterpret_cast<float*>(lds);
  const int lane = cx.lane, wv = cx.wv, w = cx.w, xc = w & 7, idx = w >> 3, kvh = 2 * xc + (idx >> 4), jg = idx & 15;
  const int r = lane >> 4, c = lane & 15, jb = (lane >> 3) & 1, jj = lane & 7;
  const int fs = 18 * idx + ((idx & 1) << 1), nf = (idx & 1) ? 16 : 20;
  const auto rs = cx.rs; const u32x4 mk = cx.mk; const u32 T = cx.T; char* ws = cx.ws;
  gchar* kp = cx.kp; gchar* vp = cx.vp;
  u32x4 kk[8], vv[8];
  kk[0] = k0; kk[1] = k1; kk[2] = k2;
  if constexpr (ROLE == R_RED) {
    const int sgm = wv >> 1, hf = wv & 1, cnt = cx.cnt, dB = cx.sB - cx.s0;
    if (sgm == 0 || cnt > dB) {
      const int vB = min(16, (16 * (dB + 1) + cnt - 1) / cnt - 1);
      const float* rp = ldf + L_REC + lane * 4 + 2 * hf;
      f32x2 v[16];
#pragma unroll
      for (int k = 0; k < 16; ++k) v[k] = *reinterpret_cast<const f32x2*>(rp + k * 256);
      f32x2 s = *reinterpret_cast<const f32x2*>(rp + 16 * 256);
      if (sgm == 1 || vB == 16 || ((cnt * vB) >> 4) >= dB) s = f32x2{0.f, 0.f};
      const u32 sel = sgm ? (0xFFFFu << vB) : ~(0xFFFFu << vB);
#pragma unroll
      for (int k = 0; k < 16; ++k) { const float wk = (float)((sel >> k) & 1u); s[0] = fmaf(wk, v[k][0], s[0]); s[1] = fmaf(wk, v[k][1], s[1]); }
      const int beta = cx.bA + sgm, slot = idx - (288 * beta + 1) / 117;
      gstore(rs, WS_P1 + (((xc * 13 + beta) * 4 + slot) * 2 + jb) * 1024 + hf * 512 + r * 128 + jj * 16, s[0], s[1], T);
    }
    STAMP(3);
    VMW(2); kk[3] = ldnt(kp + 3072);
    VMW(2); kk[4] = ldnt(kp + 4096);
  } else { VMW(1); kk[3] = ldnt(kp + 3072); VMW(1); kk[4] = ldnt(kp + 4096); }
  if constexpr (ROLE == R_QG) {
    const int beta = 2 * (idx >> 4) + wv - 4, ns = (288 * beta + 287) / 117 - (288 * beta + 1) / 117 + 1;
    const u32 off = WS_P1 + ((xc * 13 + beta) * 8 + (jg >> 3)) * 1024 + lane * 16;
    u32x4 g[4];
    VMW(1);
#define LOADS_ _Pragma("unroll") for (int k = 0; k < 4; ++k) g[k] = gload(rs, off + min(k, ns - 1) * 2048);
    LOADS_
    kk[5] = ldnt(kp + 5120); kk[6] = ldnt(kp + 6144);
    CLOBBER();
    RETRY_ALL(LOADS_, gok(g[0], T) && gok(g[1], T) && gok(g[2], T) && gok(g[3], T), 8);
#undef LOADS_
    float v0 = __uint_as_float(g[0][0]) + __uint_as_float(g[1][0]) + __uint_as_float(g[2][0]);
    float v1 = __uint_as_float(g[0][2]) + __uint_as_float(g[1][2]) + __uint_as_float(g[2][2]);
    if (ns == 4) { v0 += __uint_as_float(g[3][0]); v1 += __uint_as_float(g[3][2]); }
    const u32 pk = pkbf(v0, v1);
    u16* qd = reinterpret_cast<u16*>(lds + L_Q) + (wv - 4) * 128 + 32 * ((lane >> 3) & 3) + 16 * (lane >> 5) + jj;
    qd[0] = (u16)pk; qd[8] = (u16)(pk >> 16);
    STAMP(3);
  } else if constexpr (ROLE == R_RED) {
    VMW(2); kk[5] = ldnt(kp + 5120);
    VMW(2); kk[6] = ldnt(kp + 6144);
  } else {
    VMW(1); kk[5] = ldnt(kp + 5120);
    VMW(1); kk[6] = ldnt(kp + 6144);
  }
  VMW(1); kk[7] = ldnt(kp + 7168);
  if constexpr (ROLE == R_HG) {
    const int hb = lane >> 5, fi = min(lane & 31, nf - 1), f = fs + fi, cb = f >> 6, m = (f & 63) >> 3;
    const int bg = 4 + cb, bu = 4 + (cb + 9 - UPROT) % 9;
    const int ng = (288 * bg + 287) / 117 - (288 * bg + 1) / 117 + 1, nu = (288 * bu + 287) / 117 - (288 * bu + 1) / 117 + 1;
    const u32 off = WS_P1 + (xc * 13 * 8 + hb) * 1024 + ((m & 3) >> 1) * 512 + (m >> 2) * 128 + (f & 7) * 16;
    u32x4 g[4], u[4];
#define LOADS_ _Pragma("unroll") for (int k = 0; k < 4; ++k) { g[k] = gload(rs, off + bg * 8192 + min(k, ng - 1) * 2048); u[k] = gload(rs, off + 256 + bu * 8192 + min(k, nu - 1) * 2048); }
    VMW(1);
    LOADS_
    vv[0] = ldnt(vp); vv[1] = ldnt(vp + 1024);
    CLOBBER();
    RETRY_ALL(LOADS_, gok(g[0], T) && gok(g[1], T) && gok(g[2], T) && gok(g[3], T) && gok(u[0], T) && gok(u[1], T) && gok(u[2], T) && gok(u[3], T), 8);
#undef LOADS_
    float g0 = __uint_as_float(g[0][0]) + __uint_as_float(g[1][0]) + __uint_as_float(g[2][0]), g2 = __uint_as_float(g[0][2]) + __uint_as_float(g[1][2]) + __uint_as_float(g[2][2]);
    float u0 = __uint_as_float(u[0][0]) + __uint_as_float(u[1][0]) + __uint_as_float(u[2][0]), u2 = __uint_as_float(u[0][2]) + __uint_as_float(u[1][2]) + __uint_as_float(u[2][2]);
    if (ng == 4) { g0 += __uint_as_float(g[3][0]); g2 += __uint_as_float(g[3][2]); }
    if (nu == 4) { u0 += __uint_as_float(u[3][0]); u2 += __uint_as_float(u[3][2]); }
    asm volatile("" : "+v"(g0), "+v"(g2), "+v"(u0), "+v"(u2));
    const float h = rbf((m & 1) ? g2 : g0) * rbf((m & 1) ? u2 : u0);
    if ((lane & 31) < nf) lds[L_H + (hb * 4 + (fi & 3)) * 8 + (fi >> 2)] = pkbf(h, h);
    STAMP(3);
  } else {
    VMW(1); vv[0] = ldnt(vp);
    VMW(1); vv[1] = ldnt(vp + 1024);
  }
  VMW(1); vv[2] = ldnt(vp + 2048);
  vv[3] = ldnt(vp + 3072);
  BAR();
  STAMP(4);
  u32 bb[8];
  float Mw = 0.f, Lw = 0.f;
  {
    const u32x4 q0 = *reinterpret_cast<const u32x4*>(lds + L_Q + 4 * c), q1 = *reinterpret_cast<const u32x4*>(lds + L_Q + 64 + 4 * c);
    float x[16];
#pragma unroll
    for (int i = 0; i < 8; ++i) {
      float d0 = 0.f, d1 = 0.f;
#pragma unroll
      for (int k = 0; k < 4; ++k) { d0 = dot2(kk[i][k], q0[k], d0); d1 = dot2(kk[i][k], q1[k], d1); }
      x[2 * i] = d0; x[2 * i + 1] = d1;
    }
#define TR_STEP(HALF, CTRL) _Pragma("unroll") for (int h = 0; h < (HALF); ++h) { \
      const float t1_ = x[h] + dpp<CTRL>(x[h]), t2_ = x[h + (HALF)] + dpp<CTRL>(x[h + (HALF)]); x[h] = (c & (HALF)) ? t2_ : t1_; }
    __builtin_amdgcn_sched_barrier(0);
    VMW(2); vv[4] = ldnt(vp + 4096);
    __builtin_amdgcn_sched_barrier(0);
    TR_STEP(8, ROR8) TR_STEP(4, XOR7) TR_STEP(2, XOR2) TR_STEP(1, XOR1)
#undef TR_STEP
    const float sc = rbf(rbf(x[0]) * 0.08838834764831845f);
    float m = fmaxf(sc, dpp<XOR2>(sc)); m = fmaxf(m, dpp<ROR4>(m)); m = fmaxf(m, dpp<ROR8>(m)); m = x32max(x16max(m));
    if (lane < 2) ldf[L_M + lane * 16 + wv] = m;
    VMW(2); vv[5] = ldnt(vp + 5120);
    BAR();
    STAMP(5);
    float M;
    {
      const f32x4* mp = reinterpret_cast<const f32x4*>(ldf + L_M + (c & 1) * 16);
      const f32x4 m0 = mp[0], m1 = mp[1], m2 = mp[2], m3 = mp[3];
      M = fmaxf(fmaxf(fmaxf(fmaxf(m0[0], m0[1]), fmaxf(m0[2], m0[3])), fmaxf(fmaxf(m1[0], m1[1]), fmaxf(m1[2], m1[3]))),
                fmaxf(fmaxf(fmaxf(m2[0], m2[1]), fmaxf(m2[2], m2[3])), fmaxf(fmaxf(m3[0], m3[1]), fmaxf(m3[2], m3[3]))));
    }
    const float p = __builtin_amdgcn_exp2f((sc - M) * 1.4426950408889634f);
    float l = p + dpp<XOR2>(p); l += dpp<ROR4>(l); l += dpp<ROR8>(l); l = x32sum(x16sum(l));
    if (lane < 2) ldf[L_L + lane * 16 + wv] = l;
    BAR();
    if constexpr (ROLE == R_ML) {
      Mw = M; Lw = ldf[L_L + (lane & 1) * 16 + (lane >> 1 & 15)];
      Lw += dpp<XOR2>(Lw); Lw += dpp<ROR4>(Lw); Lw += dpp<ROR8>(Lw); Lw = x16sum(Lw);
    }
    const int pb = (int)pkbf(p, p);
#define PBC(I) { int t_ = __builtin_amdgcn_update_dpp(0, pb, 0x150 + 2 * (I), 0xf, 0x3, false); bb[I] = (u32)__builtin_amdgcn_update_dpp(t_, pb, 0x151 + 2 * (I), 0xf, 0xc, false); }
    PBC(0) PBC(1) PBC(2) PBC(3) PBC(4) PBC(5) PBC(6) PBC(7)
#undef PBC
  }
  f32x4 acc = {0.f, 0.f, 0.f, 0.f};
#pragma unroll
  for (int i = 0; i < 4; ++i) acc = rowmma(vv[i], bb[i], mk, acc);
  __builtin_amdgcn_sched_barrier(0);
  if constexpr (ROLE == R_ML) {
    vv[6] = ldnt(vp + 6144); vv[7] = ldnt(vp + 7168);
    CLOBBER();
    gstore(rs, lane < 2 ? WS_ML + (kvh * 16 + jg) * 128 + lane * 16 : 0xFFFFFF00u, Mw, Lw, T);
    CLOBBER();
    acc = rowmma(vv[4], bb[4], mk, acc); acc = rowmma(vv[5], bb[5], mk, acc);
  } else {
#pragma unroll
    for (int i = 4; i < 6; ++i) { VMW(1); vv[i + 2] = ldnt(vp + (i + 2) * 1024); CLOBBER(); acc = rowmma(vv[i], bb[i], mk, acc); __builtin_amdgcn_sched_barrier(0); }
  }
  const int gam = 4 * w + ((lane >> 1) & 3);
  const int gl = gam < 512 ? (gam & 63) : (gam & 31), ghf = gam < 512 ? ((gam >> 6) & 1) : ((gam >> 5) & 1);
  const int oidx = ((gl >> 3) & 1) * DM + (gam < 512 ? 128 * (gam >> 7) : 512) + 32 * (gl >> 4) + 16 * ghf + (gl & 7) + 8 * (lane & 1);
  u32 xres = 0;
#define PV_TAIL() do { acc = rowmma(vv[6], bb[6], mk, acc); acc = rowmma(vv[7], bb[7], mk, acc); \
    *reinterpret_cast<f32x4*>(ldf + L_REC + wv * 256 + lane * 4) = acc; STAMP(6); BAR(); \
    const float* rp_ = ldf + L_REC + (lane >> 3) * 256 + (((wv >> 1) & 3) * 16 + (wv >> 3) * 8 + jj) * 4 + 2 * (wv & 1); \
    f32x2 s_ = *reinterpret_cast<const f32x2*>(rp_) + *reinterpret_cast<const f32x2*>(rp_ + 8 * 256); \
    s_[0] = x32sum(x16sum(s_[0] + dpp<ROR8>(s_[0]))); s_[1] = x32sum(x16sum(s_[1] + dpp<ROR8>(s_[1]))); \
    if (lane < 8) gstore(rs, WS_PO + ((kvh * 16 + wv) * 16 + jg) * 128 + jj * 16, s_[0], s_[1], T); STAMP(7); } while (0)
  if constexpr (ROLE == R_L2) {
    gchar* dp = (gchar*)cx.Wd + (size_t)(576 * xc + fs + r) * 1152 + 256 * (wv - 11) + 16 * c;
    gchar* op = (gchar*)cx.Wo + (size_t)(256 * kvh + 16 * jg + r) * 1152 + 256 * (wv - 11) + 16 * c;
    u32x4 ah[5], ao[4];
    VMW(1);
    ah[0] = ldnt(dp); ah[1] = ldnt(dp + 4608); ah[2] = ldnt(dp + 2 * 4608);
    CLOBBER();
    PV_TAIL();
    ah[3] = ldnt(dp + 3 * 4608);
    ah[4] = ldnt(dp + (nf == 20 ? 4 : 3) * 4608);
#pragma unroll
    for (int t = 0; t < 4; ++t) ao[t] = ldnt(op + t * 4608);
    CLOBBER();
    STAMP(8);
    const u32x4 hb = *reinterpret_cast<const u32x4*>(lds + L_H + (jb * 4 + r) * 8);
    const u32 hb4 = nf == 20 ? lds[L_H + (jb * 4 + r) * 8 + 4] : 0u;
    f32x4 accA = {0.f, 0.f, 0.f, 0.f};
#pragma unroll
    for (int t = 0; t < 4; ++t) accA = rowmma(ah[t], hb[t], mk, accA);
    accA = rowmma(ah[4], hb4, mk, accA);
    asm volatile("s_waitcnt vmcnt(0)" : "+v"(accA) :: "memory");
    STAMP(10);
    BAR();
    const u32x4 ob = *reinterpret_cast<const u32x4*>(lds + L_O + (jb * 4 + r) * 4);
#pragma unroll
    for (int t = 0; t < 4; ++t) accA = rowmma(ao[t], ob[t], mk, accA);
    const u32 off = WS_PF + w * 9216 + (wv - 11) * 2048 + lane * 16;
    gstore(rs, off, accA[0], accA[1], T);
    gstore(rs, off + 1024, accA[2], accA[3], T);
    STAMP(9);
  } else if constexpr (ROLE == R_L2H) {
    gchar* dp = (gchar*)cx.Wd + (size_t)(576 * xc + fs + r) * 1152 + 1024 + 16 * jj;
    gchar* op = (gchar*)cx.Wo + (size_t)(256 * kvh + 16 * jg + r + 4 * jb) * 1152 + 1024 + 16 * jj;
    u32x4 ah[3], ao[2];
    VMW(1);
    ah[0] = ldnt(dp + jb * 4608); ah[1] = ldnt(dp + jb * 4608 + 9216);
    CLOBBER();
    PV_TAIL();
    ah[2] = ldnt(dp + (nf == 20 ? 4 : 3) * 4608);
    ao[0] = ldnt(op); ao[1] = ldnt(op + 9216);
    CLOBBER();
    STAMP(8);
    const u32x4 hb = *reinterpret_cast<const u32x4*>(lds + L_H + (jb * 4 + r) * 8);
    const u32 hb4 = nf == 20 ? lds[L_H + (jb * 4 + r) * 8 + 4] : 0u;
    f32x4 accA = {0.f, 0.f, 0.f, 0.f}, accB = {0.f, 0.f, 0.f, 0.f};
    accA = rowmma(ah[0], hb[0], mk, accA); accB = rowmma(ah[0], hb[1], mk, accB);
    accA = rowmma(ah[1], hb[2], mk, accA); accB = rowmma(ah[1], hb[3], mk, accB);
    accA = rowmma(ah[2], hb4, mk, accA);
    asm volatile("s_waitcnt vmcnt(0)" : "+v"(accA), "+v"(accB) :: "memory");
    STAMP(10);
    BAR();
    const u32x4 ob = *reinterpret_cast<const u32x4*>(lds + L_O + (jb * 4 + r) * 4);
    accA = rowmma(ao[0], ob[0], mk, accA); accB = rowmma(ao[0], ob[1], mk, accB);
    accA = rowmma(ao[1], ob[2], mk, accA); accB = rowmma(ao[1], ob[3], mk, accB);
    float v[4];
#pragma unroll
    for (int k = 0; k < 4; ++k) v[k] = x32sum(lane < 32 ? accA[k] : accB[k]);
    gstore(rs, WS_PF + w * 9216 + 8192 + lane * 16, lane < 32 ? v[0] : v[2], lane < 32 ? v[1] : v[3], T);
    STAMP(9);
  } else {
    PV_TAIL();
    if constexpr (ROLE == R_HG) { if (w < 144) xres = cx.X[oidx]; }
    if constexpr (ROLE == R_ML) {
      const u32 oo = WS_PO + ((kvh * 16 + jg) * 16 + c) * 128 + r * 16, mo = WS_ML + (kvh * 16 + c) * 128 + (jg >> 3) * 16;
      u32x4 p0, p1, ml;
      __builtin_amdgcn_s_sleep(ODELAY);
      CLOBBER();
#define LOADS_ { p0 = gload(rs, oo); p1 = gload(rs, oo + 64); ml = gload(rs, mo); }
      LOADS_
      RETRY(LOADS_, gok(p0, T) && gok(p1, T) && gok(ml, T), PSLEEP);
#undef LOADS_
      STAMP(11);
      const float mm = __uint_as_float(ml[0]);
      float M = fmaxf(mm, dpp<XOR1>(mm)); M = fmaxf(M, dpp<XOR2>(M)); M = fmaxf(M, dpp<XOR7>(M));
      const float e = __builtin_amdgcn_exp2f((mm - M) * 1.4426950408889634f);
      float o[5] = {e * __uint_as_float(p0[0]), e * __uint_as_float(p1[0]), e * __uint_as_float(p0[2]), e * __uint_as_float(p1[2]), e * __uint_as_float(ml[2])};
#pragma unroll
      for (int t = 0; t < 5; ++t) { o[t] += dpp<XOR1>(o[t]); o[t] += dpp<XOR2>(o[t]); o[t] += dpp<XOR7>(o[t]); }
      const float inv = __builtin_amdgcn_rcpf(o[4]);
      u32x4 ob;
#pragma unroll
      for (int t = 0; t < 4; ++t) { const float y = o[t] * inv; ob[t] = pkbf(y, y); }
      if (jj == 0) *reinterpret_cast<u32x4*>(lds + L_O + (jb * 4 + r) * 4) = ob;
    }
    BAR();
  }
  if (w >= 144) { STAMP_DUMP(); return; }
  BAR();
  STAMP(12);
  __builtin_amdgcn_s_sleep(FDELAY);
  {
    const u32 off = WS_PF + (wv * 16 + (lane >> 2)) * 9216 + (4 * w + (lane & 3)) * 16;
    u32x4 g = gload(rs, off);
    RETRY(g = gload(rs, off), gok(g, T), FSLEEP);
    STAMP(13);
    float v0 = __uint_as_float(g[0]), v1 = __uint_as_float(g[2]);
    v0 += dpp<ROR4>(v0); v1 += dpp<ROR4>(v1); v0 += dpp<ROR8>(v0); v1 += dpp<ROR8>(v1);
    v0 = x32sum(x16sum(v0)); v1 = x32sum(x16sum(v1));
    if (lane < 4) *reinterpret_cast<float2*>(ldf + L_FIN + wv * 8 + lane * 2) = make_float2(v0, v1);
    BAR();
    if constexpr (ROLE == R_HG) {
      float s = ldf[L_FIN + (lane >> 3) * 16 + jj] + ldf[L_FIN + (lane >> 3) * 16 + 8 + jj];
      s = x32sum(x16sum(s + dpp<ROR8>(s)));
      if (lane < 8) cx.out[oidx] = (u16)pkbf(bf2f(xres) + s, 0.f);
      if (w == 0 && lane == 0) __builtin_amdgcn_raw_buffer_store_b32(T, rs, WS_E, 0, 16);
      STAMP(14);
    }
  }
  STAMP_DUMP();
}

__global__ void __launch_bounds__(1024, 4) fused_decode(const u16* __restrict__ X, const u16* __restrict__ Kc, const u16* __restrict__ Vc,
                                                        const u16* __restrict__ Wq, const u16* __restrict__ Wo, const u16* __restrict__ Wg,
                                                        const u16* __restrict__ Wu, const u16* __restrict__ Wd, u16* __restrict__ out, char* ws) {
  __shared__ __attribute__((aligned(16))) u32 lds[L_TOTAL];
  float* ldf = reinterpret_cast<float*>(lds);
  const int tid = threadIdx.x, lane = tid & 63, wv = __builtin_amdgcn_readfirstlane(tid >> 6);
  const int w = blockIdx.x, xc = w & 7, idx = w >> 3, kvh = 2 * xc + (idx >> 4), jg = idx & 15;
  const int r = lane >> 4, c = lane & 15, jb = (lane >> 3) & 1, jj = lane & 7;
  asm volatile("" :: "s"(X), "s"(Kc), "s"(Vc), "s"(Wq), "s"(Wo), "s"(Wg), "s"(Wu), "s"(Wd), "s"(out), "s"(ws));
  STAMP_INIT();
  STAMP(0);
  u32 xr[5];
  if (wv < 2) {
    const u32* xp = reinterpret_cast<const u32*>(X) + wv * 288 + lane;
#pragma unroll
    for (int t = 0; t < 4; ++t) xr[t] = xp[64 * t];
    xr[4] = xp[lane < 32 ? 256 : 0];
  }
  const int s0 = (117 * idx) >> 1, cnt = 58 + (idx & 1), bA = s0 / 144, sB = 144 * (bA + 1);
  const int t0 = s0 + ((cnt * wv) >> 4), t1 = s0 + ((cnt * (wv + 1)) >> 4);
  u32x4 a[4];
  {
    gchar* q0 = (gchar*)Wq + (r * 4096 + 512 * xc + 8 * c) * 2;
    gchar* g0 = (gchar*)(c < 8 ? Wg : Wu) + (r * FF + 576 * xc + 8 * (c & 7)) * 2;
    gchar* pA = bA < 4 ? q0 + 256 * bA : g0 + 128 * (c < 8 ? bA - 4 : (bA - 4 + UPROT) % 9);
    gchar* pB = bA < 3 ? q0 + 256 * (bA + 1) : g0 + 128 * (c < 8 ? bA - 3 : (bA - 3 + UPROT) % 9);
    const u32 strA = bA < 4 ? 32768 : 36864, strB = bA < 3 ? 32768 : 36864;
#pragma unroll
    for (int i = 0; i < 4; ++i) {
      const int sg = t0 + i;
      const u32 off = sg < sB ? (sg - 144 * bA) * strA : (sg - sB) * strB;
      gchar* p = (sg < sB ? pA : pB) + off;
      if (i < 3 || sg < t1) a[i] = ldnt(p);
    }
  }
  const size_t kvo = ((size_t)(((jg >> 3) * 16 + kvh) * SK + (jg & 7) * 512 + wv * 32)) * 256 + lane * 16;
  gchar* kp = (gchar*)Kc + kvo;
  const u32x4 k0 = ldnt(kp);
  CLOBBER();
  const u32 T = *reinterpret_cast<const __attribute__((address_space(4))) u32*>((uintptr_t)ws + WS_E) + 1u;
  const u32 mword = (jj & 1) ? 0xFFFF0000u : 0x0000FFFFu;
  const u32x4 mk = {(jj >> 1) == 0 ? mword : 0u, (jj >> 1) == 1 ? mword : 0u, (jj >> 1) == 2 ? mword : 0u, (jj >> 1) == 3 ? mword : 0u};
  if (wv < 2) {
    float v[10], s = 0.f;
#pragma unroll
    for (int t = 0; t < 5; ++t) { v[2 * t] = bf2f(xr[t] & 0xffffu); v[2 * t + 1] = __uint_as_float(xr[t] & 0xffff0000u); }
#pragma unroll
    for (int t = 0; t < 8; ++t) s += v[t];
    if (lane < 32) s += v[8] + v[9];
    const float mean = wavesum(s) * (1.f / DM);
    float q = 0.f;
#pragma unroll
    for (int t = 0; t < 8; ++t) { const float d = v[t] - mean; q = fmaf(d, d, q); }
    if (lane < 32) { const float d = v[8] - mean, e = v[9] - mean; q = fmaf(d, d, fmaf(e, e, q)); }
    const float mb = rbf(mean), den = rbf(__builtin_sqrtf(rbf(rbf(wavesum(q) * (1.f / DM)) + 1e-5f)));
    u32* xd = lds + L_XN + wv * 580 + 2 * lane;
#pragma unroll
    for (int t = 0; t < 5; ++t) {
      const float y0 = rbf(v[2 * t] - mb) / den, y1 = rbf(v[2 * t + 1] - mb) / den;
      uint2 o = {pkbf(y0, y0), pkbf(y1, y1)};
      if (t < 4 || lane < 32) *reinterpret_cast<uint2*>(xd + 128 * t) = o;
    }
  }
  BAR();
  STAMP(1);
  VMW(2);
  const u32x4 k1 = ldnt(kp + 1024);
  CLOBBER();
  {
    f32x4 acc = {0.f, 0.f, 0.f, 0.f};
    const u32* xb = lds + L_XN + jb * 580 + r;
#pragma unroll
    for (int i = 0; i < 4; ++i) {
      const int sg = t0 + i;
      if (i > 0 && sg == sB && sg < t1) { *reinterpret_cast<f32x4*>(ldf + L_REC + 16 * 256 + lane * 4) = acc; acc = f32x4{0.f, 0.f, 0.f, 0.f}; }
      if (i < 3 || sg < t1) acc = rowmma(a[i], xb[4 * (sg < sB ? sg - 144 * bA : sg - sB)], mk, acc);
    }
    *reinterpret_cast<f32x4*>(ldf + L_REC + wv * 256 + lane * 4) = acc;
  }
  const u32x4 k2 = ldnt(kp + 2048);
  BAR();
  STAMP(2);
  Ctx cx;
  cx.X = X; cx.Wo = Wo; cx.Wd = Wd; cx.out = out; cx.ws = ws; cx.kp = kp; cx.vp = (gchar*)Vc + kvo; cx.lds = lds;
  cx.rs = __builtin_amdgcn_make_buffer_rsrc((void*)ws, 0, WS_END, 0x00020000); cx.mk = mk; cx.T = T;
  cx.lane = lane; cx.wv = wv; cx.w = w; cx.s0 = s0; cx.cnt = cnt; cx.bA = bA; cx.sB = sB;
  if (wv < 4) rest<R_RED>(cx, k0, k1, k2);
  else if (wv < 6) rest<R_QG>(cx, k0, k1, k2);
  else if (wv == 6) rest<R_HG>(cx, k0, k1, k2);
  else if (wv == 7) rest<R_ML>(cx, k0, k1, k2);
  else if (wv < 11) rest<R_PLAIN>(cx, k0, k1, k2);
  else if (wv < 15) rest<R_L2>(cx, k0, k1, k2);
  else rest<R_L2H>(cx, k0, k1, k2);
}

extern "C" void kernel_launch(void* const* d_in, const int* in_sizes, int n_in, void* d_out, int out_size, void* d_ws, size_t ws_size,
                              hipStream_t stream) {
  fused_decode<<<256, 1024, 0, stream>>>((const u16*)d_in[0], (const u16*)d_in[1], (const u16*)d_in[2], (const u16*)d_in[3], (const u16*)d_in[4],
                                         (const u16*)d_in[6], (const u16*)d_in[7], (const u16*)d_in[8], (u16*)d_out, (char*)d_ws);
}
```

```cpp
#define PROBE 0
#include <hip/hip_runtime.h>
#include <hip/hip_bf16.h>
#include <stdint.h>

typedef unsigned short u16;
typedef unsigned int u32;
typedef unsigned long long u64;
using u32x4 = __attribute__((ext_vector_type(4))) unsigned;
using u32x3 = __attribute__((ext_vector_type(3))) unsigned;
using f32x4 = __attribute__((ext_vector_type(4))) float;
using f32x2 = __attribute__((ext_vector_type(2))) float;
using bf16x8 = __attribute__((ext_vector_type(8))) __bf16;
using bf16x2 = __attribute__((ext_vector_type(2))) __bf16;
typedef __attribute__((address_space(1))) const u32x4 gvec;
typedef __attribute__((address_space(1))) const char gchar;

constexpr int DM = 576, SK = 4096, FF = 4608;
constexpr u32 WS_E = 0;
constexpr u32 WS_ML = 4096;
constexpr u32 WS_PO = 65536;
constexpr u32 WS_P1 = WS_PO + 524288;
constexpr u32 WS_PF = WS_P1 + 8 * 13 * 4 * 2048;
constexpr u32 WS_END = WS_PF + 256 * 9216;
constexpr u32 SPIN_CAP = 1u << 20;

constexpr int L_XN = 0;
constexpr int L_Q = 1168;
constexpr int L_H = 1296;
constexpr int L_O = 1360;
constexpr int L_M = 1392;
constexpr int L_L = 1424;
constexpr int L_FIN = 1456;
constexpr int L_REC = 1600;
constexpr int L_ST = L_REC + 17 * 256;
constexpr int L_TOTAL = L_ST + 64;
#define STAMP(id) do { if (w == PROBE_WG && wv == 6 && ((id) == 0 || (id) == PROBE)) { u64 t_ = __builtin_amdgcn_s_memrealtime(); u64 c_ = __builtin_amdgcn_s_memtime(); if (lane == 0) { *(u64*)(lds + L_ST + ((id) == 0 ? 0 : 2)) = t_; *(u64*)(lds + L_ST + ((id) == 0 ? 4 : 6)) = c_; } } } while (0)
#define STAMP_INIT()
#define STAMP_DUMP() do { if (w == PROBE_WG && wv == 6) { asm volatile("s_waitcnt lgkmcnt(0)" ::: "memory"); u64 t0_ = *(volatile u64*)(lds + L_ST), t1_ = *(volatile u64*)(lds + L_ST + 2), c0_ = *(volatile u64*)(lds + L_ST + 4), c1_ = *(volatile u64*)(lds + L_ST + 6); \
   u64 val_ = PROBE == 0 ? 0 : PROBE_CLK ? (c1_ - c0_) * 100 / (t1_ - t0_) / 10 : (t1_ - t0_); u64 tgt_ = t0_ + 6000 + 10 * val_; \
   for (int i_ = 0; i_ < 2000000 && __builtin_amdgcn_s_memrealtime() < tgt_; ++i_) __builtin_amdgcn_s_sleep(1); } } while (0)
#define PROBE_CLK 0
#define PROBE_WG 0
#define FSLEEP 4
#define PSLEEP 4
#define ODELAY 12
#define FDELAY 10
#define UPROT 4
#define WMID 2

__device__ __forceinline__ float bf2f(u32 x) { return __uint_as_float(x << 16); }
__device__ __forceinline__ float rbf(float x) { return (float)(__bf16)x; }
__device__ __forceinline__ u32 pkbf(float lo, float hi) { bf16x2 v = {(__bf16)lo, (__bf16)hi}; return __builtin_bit_cast(u32, v); }
__device__ __forceinline__ u32x4 ldnt(gchar* p) { return __builtin_nontemporal_load((gvec*)p); }
__device__ __forceinline__ float dot2(u32 a, u32 b, float c) {
  return __builtin_amdgcn_fdot2_f32_bf16(__builtin_bit_cast(bf16x2, a), __builtin_bit_cast(bf16x2, b), c, false);
}
template <int CTRL> __device__ __forceinline__ float dpp(float x) {
  return __builtin_bit_cast(float, __builtin_amdgcn_mov_dpp(__builtin_bit_cast(int, x), CTRL, 0xf, 0xf, true));
}
constexpr int XOR1 = 0xB1, XOR2 = 0x4E, XOR7 = 0x141, ROR8 = 0x128, ROR4 = 0x124;
__device__ __forceinline__ float x16sum(float x) {
  auto s = __builtin_amdgcn_permlane16_swap(__float_as_uint(x), __float_as_uint(x), false, false);
  return __uint_as_float(s[0]) + __uint_as_float(s[1]);
}
__device__ __forceinline__ float x32sum(float x) {
  auto s = __builtin_amdgcn_permlane32_swap(__float_as_uint(x), __float_as_uint(x), false, false);
  return __uint_as_float(s[0]) + __uint_as_float(s[1]);
}
__device__ __forceinline__ float x16max(float x) {
  auto s = __builtin_amdgcn_permlane16_swap(__float_as_uint(x), __float_as_uint(x), false, false);
  return fmaxf(__uint_as_float(s[0]), __uint_as_float(s[1]));
}
__device__ __forceinline__ float x32max(float x) {
  auto s = __builtin_amdgcn_permlane32_swap(__float_as_uint(x), __float_as_uint(x), false, false);
  return fmaxf(__uint_as_float(s[0]), __uint_as_float(s[1]));
}
__device__ __forceinline__ float wavesum(float s) {
  s += dpp<XOR1>(s); s += dpp<XOR2>(s); s += dpp<XOR7>(s); s += dpp<ROR8>(s); return x32sum(x16sum(s));
}
__device__ __forceinline__ f32x4 rowmma(u32x4 a, u32 bb, u32x4 mk, f32x4 acc) {
  u32x4 b = {bb & mk[0], bb & mk[1], bb & mk[2], bb & mk[3]};
  return __builtin_amdgcn_mfma_f32_16x16x32_bf16(__builtin_bit_cast(bf16x8, a), __builtin_bit_cast(bf16x8, b), acc, 0, 0, 0);
}
#define CLOBBER() asm volatile("" ::: "memory")
#define BAR() do { asm volatile("s_waitcnt lgkmcnt(0)" ::: "memory"); __builtin_amdgcn_s_barrier(); asm volatile("" ::: "memory"); } while (0)
#define VMW_(n) asm volatile("s_waitcnt vmcnt(" #n ")" ::: "memory")
#define VMW(n) VMW_(n)
#define RETRY(LOADS, OK, SL) do { if (!__all(OK)) { _Pragma("unroll 1") for (u32 spins = 0; spins < SPIN_CAP; ++spins) { \
      __builtin_amdgcn_s_sleep(SL); CLOBBER(); if (!(OK)) { LOADS; } if (__all(OK)) break; } } } while (0)
#define RETRY_ALL(LOADS, OK, SL) do { if (!__all(OK)) { _Pragma("unroll 1") for (u32 spins = 0; spins < SPIN_CAP; ++spins) { \
      __builtin_amdgcn_s_sleep(SL); CLOBBER(); LOADS; if (__all(OK)) break; } } } while (0)
__device__ __forceinline__ void gstore(__amdgpu_buffer_rsrc_t r, u32 off, float a, float b, u32 T) {
  u32x4 v = {__float_as_uint(a), T, __float_as_uint(b), T};
  __builtin_amdgcn_raw_buffer_store_b128(v, r, off, 0, 16);
}
__device__ __forceinline__ u32x4 gload(__amdgpu_buffer_rsrc_t r, u32 off) { return __builtin_amdgcn_raw_buffer_load_b128(r, off, 0, 16); }
__device__ __forceinline__ bool gok(u32x4 v, u32 T) { return v[1] == T && v[3] == T; }

struct Ctx {
  const u16* X; const u16* Wo; const u16* Wd; u16* out; char* ws; gchar* kp; gchar* vp; u32* lds;
  __amdgpu_buffer_rsrc_t rs; u32x4 mk; u32 T;
  int lane, wv, w, s0, cnt, bA, sB;
};
enum { R_RED, R_QG, R_HG, R_ML, R_PLAIN, R_L2, R_L2H };

template <int ROLE>
__device__ __forceinline__ void rest(const Ctx& cx, u32x4 k0, u32x4 k1, u32x4 k2) {
  u32* lds = cx.lds; float* ldf = reinterpret_cast<float*>(lds);
  const int lane = cx.lane, wv = cx.wv, w = cx.w, xc = w & 7, idx = w >> 3, kvh = 2 * xc + (idx >> 4), jg = idx & 15;
  const int r = lane >> 4, c = lane & 15, jb = (lane >> 3) & 1, jj = lane & 7;
  const int fs = 18 * idx + ((idx & 1) << 1), nf = (idx & 1) ? 16 : 20;
  const auto rs = cx.rs; const u32x4 mk = cx.mk; const u32 T = cx.T; char* ws = cx.ws;
  gchar* kp = cx.kp; gchar* vp = cx.vp;
  u32x4 kk[8], vv[8];
  kk[0] = k0; kk[1] = k1; kk[2] = k2;
  if constexpr (ROLE == R_RED) {
    const int sgm = wv >> 1, hf = wv & 1, cnt = cx.cnt, dB = cx.sB - cx.s0;
    if (sgm == 0 || cnt > dB) {
      const int vB = min(16, (16 * (dB + 1) + cnt - 1) / cnt - 1);
      const float* rp = ldf + L_REC + lane * 4 + 2 * hf;
      f32x2 v[16];
#pragma unroll
      for (int k = 0; k < 16; ++k) v[k] = *reinterpret_cast<const f32x2*>(rp + k * 256);
      f32x2 s = *reinterpret_cast<const f32x2*>(rp + 16 * 256);
      if (sgm == 1 || vB == 16 || ((cnt * vB) >> 4) >= dB) s = f32x2{0.f, 0.f};
      const u32 sel = sgm ? (0xFFFFu << vB) : ~(0xFFFFu << vB);
#pragma unroll
      for (int k = 0; k < 16; ++k) { const float wk = (float)((sel >> k) & 1u); s[0] = fmaf(wk, v[k][0], s[0]); s[1] = fmaf(wk, v[k][1], s[1]); }
      const int beta = cx.bA + sgm, slot = idx - (288 * beta + 1) / 117;
      gstore(rs, WS_P1 + (((xc * 13 + beta) * 4 + slot) * 2 + jb) * 1024 + hf * 512 + r * 128 + jj * 16, s[0], s[1], T);
    }
    STAMP(3);
    VMW(2); kk[3] = ldnt(kp + 3072);
    VMW(2); kk[4] = ldnt(kp + 4096);
  } else { VMW(1); kk[3] = ldnt(kp + 3072); VMW(1); kk[4] = ldnt(kp + 4096); }
  if constexpr (ROLE == R_QG) {
    const int beta = 2 * (idx >> 4) + wv - 4, ns = (288 * beta + 287) / 117 - (288 * beta + 1) / 117 + 1;
    const u32 off = WS_P1 + ((xc * 13 + beta) * 8 + (jg >> 3)) * 1024 + lane * 16;
    u32x4 g[4];
    VMW(1);
#define LOADS_ _Pragma("unroll") for (int k = 0; k < 4; ++k) g[k] = gload(rs, off + min(k, ns - 1) * 2048);
    LOADS_
    kk[5] = ldnt(kp + 5120); kk[6] = ldnt(kp + 6144);
    CLOBBER();
    RETRY_ALL(LOADS_, gok(g[0], T) && gok(g[1], T) && gok(g[2], T) && gok(g[3], T), 8);
#undef LOADS_
    float v0 = __uint_as_float(g[0][0]) + __uint_as_float(g[1][0]) + __uint_as_float(g[2][0]);
    float v1 = __uint_as_float(g[0][2]) + __uint_as_float(g[1][2]) + __uint_as_float(g[2][2]);
    if (ns == 4) { v0 += __uint_as_float(g[3][0]); v1 += __uint_as_float(g[3][2]); }
    const u32 pk = pkbf(v0, v1);
    u16* qd = reinterpret_cast<u16*>(lds + L_Q) + (wv - 4) * 128 + 32 * ((lane >> 3) & 3) + 16 * (lane >> 5) + jj;
    qd[0] = (u16)pk; qd[8] = (u16)(pk >> 16);
    STAMP(3);
  } else if constexpr (ROLE == R_RED) {
    VMW(2); kk[5] = ldnt(kp + 5120);
    VMW(2); kk[6] = ldnt(kp + 6144);
  } else {
    VMW(1); kk[5] = ldnt(kp + 5120);
    VMW(1); kk[6] = ldnt(kp + 6144);
  }
  VMW(1); kk[7] = ldnt(kp + 7168);
  if constexpr (ROLE == R_HG) {
    const int hb = lane >> 5, fi = min(lane & 31, nf - 1), f = fs + fi, cb = f >> 6, m = (f & 63) >> 3;
    const int bg = 4 + cb, bu = 4 + (cb + 9 - UPROT) % 9;
    const int ng = (288 * bg + 287) / 117 - (288 * bg + 1) / 117 + 1, nu = (288 * bu + 287) / 117 - (288 * bu + 1) / 117 + 1;
    const u32 off = WS_P1 + (xc * 13 * 8 + hb) * 1024 + ((m & 3) >> 1) * 512 + (m >> 2) * 128 + (f & 7) * 16;
    u32x4 g[4], u[4];
#define LOADS_ _Pragma("unroll") for (int k = 0; k < 4; ++k) { g[k] = gload(rs, off + bg * 8192 + min(k, ng - 1) * 2048); u[k] = gload(rs, off + 256 + bu * 8192 + min(k, nu - 1) * 2048); }
    VMW(1);
    LOADS_
    vv[0] = ldnt(vp); vv[1] = ldnt(vp + 1024);
    CLOBBER();
    RETRY_ALL(LOADS_, gok(g[0], T) && gok(g[1], T) && gok(g[2], T) && gok(g[3], T) && gok(u[0], T) && gok(u[1], T) && gok(u[2], T) && gok(u[3], T), 8);
#undef LOADS_
    float g0 = __uint_as_float(g[0][0]) + __uint_as_float(g[1][0]) + __uint_as_float(g[2][0]), g2 = __uint_as_float(g[0][2]) + __uint_as_float(g[1][2]) + __uint_as_float(g[2][2]);
    float u0 = __uint_as_float(u[0][0]) + __uint_as_float(u[1][0]) + __uint_as_float(u[2][0]), u2 = __uint_as_float(u[0][2]) + __uint_as_float(u[1][2]) + __uint_as_float(u[2][2]);
    if (ng == 4) { g0 += __uint_as_float(g[3][0]); g2 += __uint_as_float(g[3][2]); }
    if (nu == 4) { u0 += __uint_as_float(u[3][0]); u2 += __uint_as_float(u[3][2]); }
    asm volatile("" : "+v"(g0), "+v"(g2), "+v"(u0), "+v"(u2));
    const float h = rbf((m & 1) ? g2 : g0) * rbf((m & 1) ? u2 : u0);
    if ((lane & 31) < nf) lds[L_H + (hb * 4 + (fi & 3)) * 8 + (fi >> 2)] = pkbf(h, h);
    STAMP(3);
  } else {
    VMW(1); vv[0] = ldnt(vp);
    VMW(1); vv[1] = ldnt(vp + 1024);
  }
  VMW(1); vv[2] = ldnt(vp + 2048);
  vv[3] = ldnt(vp + 3072);
  BAR();
  STAMP(4);
  u32 bb[8];
  float Mw = 0.f, Lw = 0.f;
  {
    const u32x4 q0 = *reinterpret_cast<const u32x4*>(lds + L_Q + 4 * c), q1 = *reinterpret_cast<const u32x4*>(lds + L_Q + 64 + 4 * c);
    float x[16];
#pragma unroll
    for (int i = 0; i < 8; ++i) {
      float d0 = 0.f, d1 = 0.f;
#pragma unroll
      for (int k = 0; k < 4; ++k) { d0 = dot2(kk[i][k], q0[k], d0); d1 = dot2(kk[i][k], q1[k], d1); }
      x[2 * i] = d0; x[2 * i + 1] = d1;
    }
#define TR_STEP(HALF, CTRL) _Pragma("unroll") for (int h = 0; h < (HALF); ++h) { \
      const float t1_ = x[h] + dpp<CTRL>(x[h]), t2_ = x[h + (HALF)] + dpp<CTRL>(x[h + (HALF)]); x[h] = (c & (HALF)) ? t2_ : t1_; }
    __builtin_amdgcn_sched_barrier(0);
    VMW(2); vv[4] = ldnt(vp + 4096);
    __builtin_amdgcn_sched_barrier(0);
    TR_STEP(8, ROR8) TR_STEP(4, XOR7) TR_STEP(2, XOR2) TR_STEP(1, XOR1)
#undef TR_STEP
    const float sc = rbf(rbf(x[0]) * 0.08838834764831845f);
    float m = fmaxf(sc, dpp<XOR2>(sc)); m = fmaxf(m, dpp<ROR4>(m)); m = fmaxf(m, dpp<ROR8>(m)); m = x32max(x16max(m));
    if (lane < 2) ldf[L_M + lane * 16 + wv] = m;
    VMW(2); vv[5] = ldnt(vp + 5120);
    BAR();
    STAMP(5);
    float M;
    {
      const f32x4* mp = reinterpret_cast<const f32x4*>(ldf + L_M + (c & 1) * 16);
      const f32x4 m0 = mp[0], m1 = mp[1], m2 = mp[2], m3 = mp[3];
      M = fmaxf(fmaxf(fmaxf(fmaxf(m0[0], m0[1]), fmaxf(m0[2], m0[3])), fmaxf(fmaxf(m1[0], m1[1]), fmaxf(m1[2], m1[3]))),
                fmaxf(fmaxf(fmaxf(m2[0], m2[1]), fmaxf(m2[2], m2[3])), fmaxf(fmaxf(m3[0], m3[1]), fmaxf(m3[2], m3[3]))));
    }
    const float p = __builtin_amdgcn_exp2f((sc - M) * 1.4426950408889634f);
    float l = p + dpp<XOR2>(p); l += dpp<ROR4>(l); l += dpp<ROR8>(l); l = x32sum(x16sum(l));
    if (lane < 2) ldf[L_L + lane * 16 + wv] = l;
    BAR();
    if constexpr (ROLE == R_ML) {
      Mw = M; Lw = ldf[L_L + (lane & 1) * 16 + (lane >> 1 & 15)];
      Lw += dpp<XOR2>(Lw); Lw += dpp<ROR4>(Lw); Lw += dpp<ROR8>(Lw); Lw = x16sum(Lw);
    }
    const int pb = (int)pkbf(p, p);
#define PBC(I) { int t_ = __builtin_amdgcn_update_dpp(0, pb, 0x150 + 2 * (I), 0xf, 0x3, false); bb[I] = (u32)__builtin_amdgcn_update_dpp(t_, pb, 0x151 + 2 * (I), 0xf, 0xc, false); }
    PBC(0) PBC(1) PBC(2) PBC(3) PBC(4) PBC(5) PBC(6) PBC(7)
#undef PBC
  }
  f32x4 acc = {0.f, 0.f, 0.f, 0.f};
#pragma unroll
  for (int i = 0; i < 4; ++i) acc = rowmma(vv[i], bb[i], mk, acc);
  __builtin_amdgcn_sched_barrier(0);
  if constexpr (ROLE == R_ML) {
    vv[6] = ldnt(vp + 6144); vv[7] = ldnt(vp + 7168);
    CLOBBER();
    gstore(rs, lane < 2 ? WS_ML + (kvh * 16 + jg) * 128 + lane * 16 : 0xFFFFFF00u, Mw, Lw, T);
    CLOBBER();
    acc = rowmma(vv[4], bb[4], mk, acc); acc = rowmma(vv[5], bb[5], mk, acc);
  } else {
#pragma unroll
    for (int i = 4; i < 6; ++i) { VMW(1); vv[i + 2] = ldnt(vp + (i + 2) * 1024); CLOBBER(); acc = rowmma(vv[i], bb[i], mk, acc); __builtin_amdgcn_sched_barrier(0); }
  }
  const int gam = 4 * w + ((lane >> 1) & 3);
  const int gl = gam < 512 ? (gam & 63) : (gam & 31), ghf = gam < 512 ? ((gam >> 6) & 1) : ((gam >> 5) & 1);
  const int oidx = ((gl >> 3) & 1) * DM + (gam < 512 ? 128 * (gam >> 7) : 512) + 32 * (gl >> 4) + 16 * ghf + (gl & 7) + 8 * (lane & 1);
  u32 xres = 0;
#define PV_TAIL() do { acc = rowmma(vv[6], bb[6], mk, acc); acc = rowmma(vv[7], bb[7], mk, acc); \
    *reinterpret_cast<f32x4*>(ldf + L_REC + wv * 256 + lane * 4) = acc; STAMP(6); BAR(); \
    const float* rp_ = ldf + L_REC + (lane >> 3) * 256 + (((wv >> 1) & 3) * 16 + (wv >> 3) * 8 + jj) * 4 + 2 * (wv & 1); \
    f32x2 s_ = *reinterpret_cast<const f32x2*>(rp_) + *reinterpret_cast<const f32x2*>(rp_ + 8 * 256); \
    s_[0] = x32sum(x16sum(s_[0] + dpp<ROR8>(s_[0]))); s_[1] = x32sum(x16sum(s_[1] + dpp<ROR8>(s_[1]))); \
    if (lane < 8) gstore(rs, WS_PO + ((kvh * 16 + wv) * 16 + jg) * 128 + jj * 16, s_[0], s_[1], T); STAMP(7); } while (0)
  if constexpr (ROLE == R_L2) {
    gchar* dp = (gchar*)cx.Wd + (size_t)(576 * xc + fs + r) * 1152 + 256 * (wv - 11) + 16 * c;
    gchar* op = (gchar*)cx.Wo + (size_t)(256 * kvh + 16 * jg + r) * 1152 + 256 * (wv - 11) + 16 * c;
    u32x4 ah[5], ao[4];
    VMW(1);
    ah[0] = ldnt(dp); ah[1] = ldnt(dp + 4608); ah[2] = ldnt(dp + 2 * 4608);
    CLOBBER();
    PV_TAIL();
    ah[3] = ldnt(dp + 3 * 4608);
    ah[4] = ldnt(dp + (nf == 20 ? 4 : 3) * 4608);
#pragma unroll
    for (int t = 0; t < 4; ++t) ao[t] = ldnt(op + t * 4608);
    CLOBBER();
    STAMP(8);
    const u32x4 hb = *reinterpret_cast<const u32x4*>(lds + L_H + (jb * 4 + r) * 8);
    const u32 hb4 = nf == 20 ? lds[L_H + (jb * 4 + r) * 8 + 4] : 0u;
    f32x4 accA = {0.f, 0.f, 0.f, 0.f};
#pragma unroll
    for (int t = 0; t < 4; ++t) accA = rowmma(ah[t], hb[t], mk, accA);
    accA = rowmma(ah[4], hb4, mk, accA);
    asm volatile("s_waitcnt vmcnt(0)" : "+v"(accA) :: "memory");
    STAMP(10);
    BAR();
    const u32x4 ob = *reinterpret_cast<const u32x4*>(lds + L_O + (jb * 4 + r) * 4);
#pragma unroll
    for (int t = 0; t < 4; ++t) accA = rowmma(ao[t], ob[t], mk, accA);
    const u32 off = WS_PF + w * 9216 + (wv - 11) * 2048 + lane * 16;
    gstore(rs, off, accA[0], accA[1], T);
    gstore(rs, off + 1024, accA[2], accA[3], T);
    STAMP(9);
  } else if constexpr (ROLE == R_L2H) {
    gchar* dp = (gchar*)cx.Wd + (size_t)(576 * xc + fs + r) * 1152 + 1024 + 16 * jj;
    gchar* op = (gchar*)cx.Wo + (size_t)(256 * kvh + 16 * jg + r + 4 * jb) * 1152 + 1024 + 16 * jj;
    u32x4 ah[3], ao[2];
    VMW(1);
    ah[0] = ldnt(dp + jb * 4608); ah[1] = ldnt(dp + jb * 4608 + 9216);
    CLOBBER();
    PV_TAIL();
    ah[2] = ldnt(dp + (nf == 20 ? 4 : 3) * 4608);
    ao[0] = ldnt(op); ao[1] = ldnt(op + 9216);
    CLOBBER();
    STAMP(8);
    const u32x4 hb = *reinterpret_cast<const u32x4*>(lds + L_H + (jb * 4 + r) * 8);
    const u32 hb4 = nf == 20 ? lds[L_H + (jb * 4 + r) * 8 + 4] : 0u;
    f32x4 accA = {0.f, 0.f, 0.f, 0.f}, accB = {0.f, 0.f, 0.f, 0.f};
    accA = rowmma(ah[0], hb[0], mk, accA); accB = rowmma(ah[0], hb[1], mk, accB);
    accA = rowmma(ah[1], hb[2], mk, accA); accB = rowmma(ah[1], hb[3], mk, accB);
    accA = rowmma(ah[2], hb4, mk, accA);
    asm volatile("s_waitcnt vmcnt(0)" : "+v"(accA), "+v"(accB) :: "memory");
    STAMP(10);
    BAR();
    const u32x4 ob = *reinterpret_cast<const u32x4*>(lds + L_O + (jb * 4 + r) * 4);
    accA = rowmma(ao[0], ob[0], mk, accA); accB = rowmma(ao[0], ob[1], mk, accB);
    accA = rowmma(ao[1], ob[2], mk, accA); accB = rowmma(ao[1], ob[3], mk, accB);
    float v[4];
#pragma unroll
    for (int k = 0; k < 4; ++k) v[k] = x32sum(lane < 32 ? accA[k] : accB[k]);
    gstore(rs, WS_PF + w * 9216 + 8192 + lane * 16, lane < 32 ? v[0] : v[2], lane < 32 ? v[1] : v[3], T);
    STAMP(9);
  } else {
    PV_TAIL();
    if constexpr (ROLE == R_HG) { if (w < 144) xres = cx.X[oidx]; }
    if constexpr (ROLE == R_ML) {
      const u32 oo = WS_PO + ((kvh * 16 + jg) * 16 + c) * 128 + r * 16, mo = WS_ML + (kvh * 16 + c) * 128 + (jg >> 3) * 16;
      u32x4 p0, p1, ml;
      __builtin_amdgcn_s_sleep(ODELAY);
      CLOBBER();
#define LOADS_ { p0 = gload(rs, oo); p1 = gload(rs, oo + 64); ml = gload(rs, mo); }
      LOADS_
      RETRY(LOADS_, gok(p0, T) && gok(p1, T) && gok(ml, T), PSLEEP);
#undef LOADS_
      STAMP(11);
      const float mm = __uint_as_float(ml[0]);
      float M = fmaxf(mm, dpp<XOR1>(mm)); M = fmaxf(M, dpp<XOR2>(M)); M = fmaxf(M, dpp<XOR7>(M));
      const float e = __builtin_amdgcn_exp2f((mm - M) * 1.4426950408889634f);
      float o[5] = {e * __uint_as_float(p0[0]), e * __uint_as_float(p1[0]), e * __uint_as_float(p0[2]), e * __uint_as_float(p1[2]), e * __uint_as_float(ml[2])};
#pragma unroll
      for (int t = 0; t < 5; ++t) { o[t] += dpp<XOR1>(o[t]); o[t] += dpp<XOR2>(o[t]); o[t] += dpp<XOR7>(o[t]); }
      const float inv = __builtin_amdgcn_rcpf(o[4]);
      u32x4 ob;
#pragma unroll
      for (int t = 0; t < 4; ++t) { const float y = o[t] * inv; ob[t] = pkbf(y, y); }
      if (jj == 0) *reinterpret_cast<u32x4*>(lds + L_O + (jb * 4 + r) * 4) = ob;
    }
    BAR();
  }
  if (w >= 144) { STAMP_DUMP(); return; }
  BAR();
  STAMP(12);
  __builtin_amdgcn_s_sleep(FDELAY);
  {
    const u32 off = WS_PF + (wv * 16 + (lane >> 2)) * 9216 + (4 * w + (lane & 3)) * 16;
    u32x4 g = gload(rs, off);
    RETRY(g = gload(rs, off), gok(g, T), FSLEEP);
    STAMP(13);
    float v0 = __uint_as_float(g[0]), v1 = __uint_as_float(g[2]);
    v0 += dpp<ROR4>(v0); v1 += dpp<ROR4>(v1); v0 += dpp<ROR8>(v0); v1 += dpp<ROR8>(v1);
    v0 = x32sum(x16sum(v0)); v1 = x32sum(x16sum(v1));
    if (lane < 4) *reinterpret_cast<float2*>(ldf + L_FIN + wv * 8 + lane * 2) = make_float2(v0, v1);
    BAR();
    if constexpr (ROLE == R_HG) {
      float s = ldf[L_FIN + (lane >> 3) * 16 + jj] + ldf[L_FIN + (lane >> 3) * 16 + 8 + jj];
      s = x32sum(x16sum(s + dpp<ROR8>(s)));
      if (lane < 8) cx.out[oidx] = (u16)pkbf(bf2f(xres) + s, 0.f);
      if (w == 0 && lane == 0) __builtin_amdgcn_raw_buffer_store_b32(T, rs, WS_E, 0, 16);
      STAMP(14);
    }
  }
  STAMP_DUMP();
}

__global__ void __launch_bounds__(1024, 4) fused_decode(const u16* __restrict__ X, const u16* __restrict__ Kc, const u16* __restrict__ Vc,
                                                        const u16* __restrict__ Wq, const u16* __restrict__ Wo, const u16* __restrict__ Wg,
                                                        const u16* __restrict__ Wu, const u16* __restrict__ Wd, u16* __restrict__ out, char* ws) {
  __shared__ __attribute__((aligned(16))) u32 lds[L_TOTAL];
  float* ldf = reinterpret_cast<float*>(lds);
  const int tid = threadIdx.x, lane = tid & 63, wv = __builtin_amdgcn_readfirstlane(tid >> 6);
  const int w = blockIdx.x, xc = w & 7, idx = w >> 3, kvh = 2 * xc + (idx >> 4), jg = idx & 15;
  const int r = lane >> 4, c = lane & 15, jb = (lane >> 3) & 1, jj = lane & 7;
  asm volatile("" :: "s"(X), "s"(Kc), "s"(Vc), "s"(Wq), "s"(Wo), "s"(Wg), "s"(Wu), "s"(Wd), "s"(out), "s"(ws));
  STAMP_INIT();
  STAMP(0);
  u32 xr[5];
  if (wv < 2) {
    const u32* xp = reinterpret_cast<const u32*>(X) + wv * 288 + lane;
#pragma unroll
    for (int t = 0; t < 4; ++t) xr[t] = xp[64 * t];
    xr[4] = xp[lane < 32 ? 256 : 0];
  }
  const int s0 = (117 * idx) >> 1, cnt = 58 + (idx & 1), bA = s0 / 144, sB = 144 * (bA + 1);
  const int t0 = s0 + ((cnt * wv) >> 4), t1 = s0 + ((cnt * (wv + 1)) >> 4);
  u32x4 a[4];
  {
    gchar* q0 = (gchar*)Wq + (r * 4096 + 512 * xc + 8 * c) * 2;
    gchar* g0 = (gchar*)(c < 8 ? Wg : Wu) + (r * FF + 576 * xc + 8 * (c & 7)) * 2;
    gchar* pA = bA < 4 ? q0 + 256 * bA : g0 + 128 * (c < 8 ? bA - 4 : (bA - 4 + UPROT) % 9);
    gchar* pB = bA < 3 ? q0 + 256 * (bA + 1) : g0 + 128 * (c < 8 ? bA - 3 : (bA - 3 + UPROT) % 9);
    const u32 strA = bA < 4 ? 32768 : 36864, strB = bA < 3 ? 32768 : 36864;
#pragma unroll
    for (int i = 0; i < 4; ++i) {
      const int sg = t0 + i;
      const u32 off = sg < sB ? (sg - 144 * bA) * strA : (sg - sB) * strB;
      gchar* p = (sg < sB ? pA : pB) + off;
      if (i < 3 || sg < t1) a[i] = ldnt(p);
    }
  }
  const size_t kvo = ((size_t)(((jg >> 3) * 16 + kvh) * SK + (jg & 7) * 512 + wv * 32)) * 256 + lane * 16;
  gchar* kp = (gchar*)Kc + kvo;
  const u32x4 k0 = ldnt(kp);
  CLOBBER();
  const u32 T = *reinterpret_cast<const __attribute__((address_space(4))) u32*>((uintptr_t)ws + WS_E) + 1u;
  const u32 mword = (jj & 1) ? 0xFFFF0000u : 0x0000FFFFu;
  const u32x4 mk = {(jj >> 1) == 0 ? mword : 0u, (jj >> 1) == 1 ? mword : 0u, (jj >> 1) == 2 ? mword : 0u, (jj >> 1) == 3 ? mword : 0u};
  if (wv < 2) {
    float v[10], s = 0.f;
#pragma unroll
    for (int t = 0; t < 5; ++t) { v[2 * t] = bf2f(xr[t] & 0xffffu); v[2 * t + 1] = __uint_as_float(xr[t] & 0xffff0000u); }
#pragma unroll
    for (int t = 0; t < 8; ++t) s += v[t];
    if (lane < 32) s += v[8] + v[9];
    const float mean = wavesum(s) * (1.f / DM);
    float q = 0.f;
#pragma unroll
    for (int t = 0; t < 8; ++t) { const float d = v[t] - mean; q = fmaf(d, d, q); }
    if (lane < 32) { const float d = v[8] - mean, e = v[9] - mean; q = fmaf(d, d, fmaf(e, e, q)); }
    const float mb = rbf(mean), den = rbf(__builtin_sqrtf(rbf(rbf(wavesum(q) * (1.f / DM)) + 1e-5f)));
    u32* xd = lds + L_XN + wv * 580 + 2 * lane;
#pragma unroll
    for (int t = 0; t < 5; ++t) {
      const float y0 = rbf(v[2 * t] - mb) / den, y1 = rbf(v[2 * t + 1] - mb) / den;
      uint2 o = {pkbf(y0, y0), pkbf(y1, y1)};
      if (t < 4 || lane < 32) *reinterpret_cast<uint2*>(xd + 128 * t) = o;
    }
  }
  BAR();
  STAMP(1);
  VMW(2);
  const u32x4 k1 = ldnt(kp + 1024);
  CLOBBER();
  {
    f32x4 acc = {0.f, 0.f, 0.f, 0.f};
    const u32* xb = lds + L_XN + jb * 580 + r;
#pragma unroll
    for (int i = 0; i < 4; ++i) {
      const int sg = t0 + i;
      if (i > 0 && sg == sB && sg < t1) { *reinterpret_cast<f32x4*>(ldf + L_REC + 16 * 256 + lane * 4) = acc; acc = f32x4{0.f, 0.f, 0.f, 0.f}; }
      if (i < 3 || sg < t1) acc = rowmma(a[i], xb[4 * (sg < sB ? sg - 144 * bA : sg - sB)], mk, acc);
    }
    *reinterpret_cast<f32x4*>(ldf + L_REC + wv * 256 + lane * 4) = acc;
  }
  const u32x4 k2 = ldnt(kp + 2048);
  BAR();
  STAMP(2);
  Ctx cx;
  cx.X = X; cx.Wo = Wo; cx.Wd = Wd; cx.out = out; cx.ws = ws; cx.kp = kp; cx.vp = (gchar*)Vc + kvo; cx.lds = lds;
  cx.rs = __builtin_amdgcn_make_buffer_rsrc((void*)ws, 0, WS_END, 0x00020000); cx.mk = mk; cx.T = T;
  cx.lane = lane; cx.wv = wv; cx.w = w; cx.s0 = s0; cx.cnt = cnt; cx.bA = bA; cx.sB = sB;
  if (wv < 4) rest<R_RED>(cx, k0, k1, k2);
  else if (wv < 6) rest<R_QG>(cx, k0, k1, k2);
  else if (wv == 6) rest<R_HG>(cx, k0, k1, k2);
  else if (wv == 7) rest<R_ML>(cx, k0, k1, k2);
  else if (wv < 11) rest<R_PLAIN>(cx, k0, k1, k2);
  else if (wv < 15) rest<R_L2>(cx, k0, k1, k2);
  else rest<R_L2H>(cx, k0, k1, k2);
}

extern "C" void kernel_launch(void* const* d_in, const int* in_sizes, int n_in, void* d_out, int out_size, void* d_ws, size_t ws_size,
                              hipStream_t stream) {
  fused_decode<<<256, 1024, 0, stream>>>((const u16*)d_in[0], (const u16*)d_in[1], (const u16*)d_in[2], (const u16*)d_in[3], (const u16*)d_in[4],
                                         (const u16*)d_in[6], (const u16*)d_in[7], (const u16*)d_in[8], (u16*)d_out, (char*)d_ws);
}
```

```cpp
#include <hip/hip_runtime.h>
#include <hip/hip_bf16.h>
#include <stdint.h>

typedef unsigned short u16;
typedef unsigned int u32;
typedef unsigned long long u64;
using u32x4 = __attribute__((ext_vector_type(4))) unsigned;
using u32x3 = __attribute__((ext_vector_type(3))) unsigned;
using f32x4 = __attribute__((ext_vector_type(4))) float;
using f32x2 = __attribute__((ext_vector_type(2))) float;
using bf16x8 = __attribute__((ext_vector_type(8))) __bf16;
using bf16x2 = __attribute__((ext_vector_type(2))) __bf16;
typedef __attribute__((address_space(1))) const u32x4 gvec;
typedef __attribute__((address_space(1))) const char gchar;

constexpr int DM = 576, SK = 4096, FF = 4608;
constexpr u32 WS_E = 0;
constexpr u32 WS_ML = 4096;
constexpr u32 WS_PO = 65536;
constexpr u32 WS_P1 = WS_PO + 524288;
constexpr u32 WS_PF = WS_P1 + 8 * 13 * 4 * 2048;
constexpr u32 WS_END = WS_PF + 256 * 9216;
constexpr u32 SPIN_CAP = 1u << 20;

constexpr int L_XN = 0;
constexpr int L_Q = 1168;
constexpr int L_H = 1296;
constexpr int L_O = 1360;
constexpr int L_M = 1392;
constexpr int L_L = 1424;
constexpr int L_FIN = 1456;
constexpr int L_REC = 1600;
constexpr int L_ST = L_REC + 17 * 256;
constexpr int L_TOTAL = L_ST;
#define STAMP(id)
#define STAMP_INIT()
#define STAMP_DUMP()
#define FSLEEP 4
#define PSLEEP 4
#define ODELAY 12
#define FDELAY 10
#define UPROT 4
#define WMID 2

__device__ __forceinline__ float bf2f(u32 x) { return __uint_as_float(x << 16); }
__device__ __forceinline__ float rbf(float x) { return (float)(__bf16)x; }
__device__ __forceinline__ u32 pkbf(float lo, float hi) { bf16x2 v = {(__bf16)lo, (__bf16)hi}; return __builtin_bit_cast(u32, v); }
__device__ __forceinline__ u32x4 ldnt(gchar* p) { return *(gvec*)p; }
__device__ __forceinline__ float dot2(u32 a, u32 b, float c) {
  return __builtin_amdgcn_fdot2_f32_bf16(__builtin_bit_cast(bf16x2, a), __builtin_bit_cast(bf16x2, b), c, false);
}
template <int CTRL> __device__ __forceinline__ float dpp(float x) {
  return __builtin_bit_cast(float, __builtin_amdgcn_mov_dpp(__builtin_bit_cast(int, x), CTRL, 0xf, 0xf, true));
}
constexpr int XOR1 = 0xB1, XOR2 = 0x4E, XOR7 = 0x141, ROR8 = 0x128, ROR4 = 0x124;
__device__ __forceinline__ float x16sum(float x) {
  auto s = __builtin_amdgcn_permlane16_swap(__float_as_uint(x), __float_as_uint(x), false, false);
  return __uint_as_float(s[0]) + __uint_as_float(s[1]);
}
__device__ __forceinline__ float x32sum(float x) {
  auto s = __builtin_amdgcn_permlane32_swap(__float_as_uint(x), __float_as_uint(x), false, false);
  return __uint_as_float(s[0]) + __uint_as_float(s[1]);
}
__device__ __forceinline__ float x16max(float x) {
  auto s = __builtin_amdgcn_permlane16_swap(__float_as_uint(x), __float_as_uint(x), false, false);
  return fmaxf(__uint_as_float(s[0]), __uint_as_float(s[1]));
}
__device__ __forceinline__ float x32max(float x) {
  auto s = __builtin_amdgcn_permlane32_swap(__float_as_uint(x), __float_as_uint(x), false, false);
  return fmaxf(__uint_as_float(s[0]), __uint_as_float(s[1]));
}
__device__ __forceinline__ float wavesum(float s) {
  s += dpp<XOR1>(s); s += dpp<XOR2>(s); s += dpp<XOR7>(s); s += dpp<ROR8>(s); return x32sum(x16sum(s));
}
__device__ __forceinline__ f32x4 rowmma(u32x4 a, u32 bb, u32x4 mk, f32x4 acc) {
  u32x4 b = {bb & mk[0], bb & mk[1], bb & mk[2], bb & mk[3]};
  return __builtin_amdgcn_mfma_f32_16x16x32_bf16(__builtin_bit_cast(bf16x8, a), __builtin_bit_cast(bf16x8, b), acc, 0, 0, 0);
}
#define CLOBBER() asm volatile("" ::: "memory")
#define BAR() do { asm volatile("s_waitcnt lgkmcnt(0)" ::: "memory"); __builtin_amdgcn_s_barrier(); asm volatile("" ::: "memory"); } while (0)
#define VMW_(n) asm volatile("s_waitcnt vmcnt(" #n ")" ::: "memory")
#define VMW(n) VMW_(n)
#define RETRY(LOADS, OK, SL) do { if (!__all(OK)) { _Pragma("unroll 1") for (u32 spins = 0; spins < SPIN_CAP; ++spins) { \
      __builtin_amdgcn_s_sleep(SL); CLOBBER(); if (!(OK)) { LOADS; } if (__all(OK)) break; } } } while (0)
#define RETRY_ALL(LOADS, OK, SL) do { if (!__all(OK)) { _Pragma("unroll 1") for (u32 spins = 0; spins < SPIN_CAP; ++spins) { \
      __builtin_amdgcn_s_sleep(SL); CLOBBER(); LOADS; if (__all(OK)) break; } } } while (0)
__device__ __forceinline__ void gstore(__amdgpu_buffer_rsrc_t r, u32 off, float a, float b, u32 T) {
  u32x4 v = {__float_as_uint(a), T, __float_as_uint(b), T};
  __builtin_amdgcn_raw_buffer_store_b128(v, r, off, 0, 16);
}
__device__ __forceinline__ u32x4 gload(__amdgpu_buffer_rsrc_t r, u32 off) { return __builtin_amdgcn_raw_buffer_load_b128(r, off, 0, 16); }
__device__ __forceinline__ bool gok(u32x4 v, u32 T) { return v[1] == T && v[3] == T; }

struct Ctx {
  const u16* X; const u16* Wo; const u16* Wd; u16* out; char* ws; gchar* kp; gchar* vp; u32* lds;
  __amdgpu_buffer_rsrc_t rs; u32x4 mk; u32 T;
  int lane, wv, w, s0, cnt, bA, sB;
};
enum { R_RED, R_QG, R_HG, R_ML, R_PLAIN, R_L2, R_L2H };

template <int ROLE>
__device__ __forceinline__ void rest(const Ctx& cx, u32x4 k0, u32x4 k1, u32x4 k2) {
  u32* lds = cx.lds; float* ldf = reinterpret_cast<float*>(lds);
  const int lane = cx.lane, wv = cx.wv, w = cx.w, xc = w & 7, idx = w >> 3, kvh = 2 * xc + (idx >> 4), jg = idx & 15;
  const int r = lane >> 4, c = lane & 15, jb = (lane >> 3) & 1, jj = lane & 7;
  const int fs = 18 * idx + ((idx & 1) << 1), nf = (idx & 1) ? 16 : 20;
  const auto rs = cx.rs; const u32x4 mk = cx.mk; const u32 T = cx.T; char* ws = cx.ws;
  gchar* kp = cx.kp; gchar* vp = cx.vp;
  u32x4 kk[8], vv[8];
  kk[0] = k0; kk[1] = k1; kk[2] = k2;
  if constexpr (ROLE == R_RED) {
    const int sgm = wv >> 1, hf = wv & 1, cnt = cx.cnt, dB = cx.sB - cx.s0;
    if (sgm == 0 || cnt > dB) {
      const int vB = min(16, (16 * (dB + 1) + cnt - 1) / cnt - 1);
      const float* rp = ldf + L_REC + lane * 4 + 2 * hf;
      f32x2 v[16];
#pragma unroll
      for (int k = 0; k < 16; ++k) v[k] = *reinterpret_cast<const f32x2*>(rp + k * 256);
      f32x2 s = *reinterpret_cast<const f32x2*>(rp + 16 * 256);
      if (sgm == 1 || vB == 16 || ((cnt * vB) >> 4) >= dB) s = f32x2{0.f, 0.f};
      const u32 sel = sgm ? (0xFFFFu << vB) : ~(0xFFFFu << vB);
#pragma unroll
      for (int k = 0; k < 16; ++k) { const float wk = (float)((sel >> k) & 1u); s[0] = fmaf(wk, v[k][0], s[0]); s[1] = fmaf(wk, v[k][1], s[1]); }
      const int beta = cx.bA + sgm, slot = idx - (288 * beta + 1) / 117;
      gstore(rs, WS_P1 + (((xc * 13 + beta) * 4 + slot) * 2 + jb) * 1024 + hf * 512 + r * 128 + jj * 16, s[0], s[1], T);
    }
    STAMP(3);
    VMW(2); kk[3] = ldnt(kp + 3072);
    VMW(2); kk[4] = ldnt(kp + 4096);
  } else { VMW(1); kk[3] = ldnt(kp + 3072); VMW(1); kk[4] = ldnt(kp + 4096); }
  if constexpr (ROLE == R_QG) {
    const int beta = 2 * (idx >> 4) + wv - 4, ns = (288 * beta + 287) / 117 - (288 * beta + 1) / 117 + 1;
    const u32 off = WS_P1 + ((xc * 13 + beta) * 8 + (jg >> 3)) * 1024 + lane * 16;
    u32x4 g[4];
    VMW(1);
#define LOADS_ _Pragma("unroll") for (int k = 0; k < 4; ++k) g[k] = gload(rs, off + min(k, ns - 1) * 2048);
    LOADS_
    kk[5] = ldnt(kp + 5120); kk[6] = ldnt(kp + 6144);
    CLOBBER();
    RETRY_ALL(LOADS_, gok(g[0], T) && gok(g[1], T) && gok(g[2], T) && gok(g[3], T), 8);
#undef LOADS_
    float v0 = __uint_as_float(g[0][0]) + __uint_as_float(g[1][0]) + __uint_as_float(g[2][0]);
    float v1 = __uint_as_float(g[0][2]) + __uint_as_float(g[1][2]) + __uint_as_float(g[2][2]);
    if (ns == 4) { v0 += __uint_as_float(g[3][0]); v1 += __uint_as_float(g[3][2]); }
    const u32 pk = pkbf(v0, v1);
    u16* qd = reinterpret_cast<u16*>(lds + L_Q) + (wv - 4) * 128 + 32 * ((lane >> 3) & 3) + 16 * (lane >> 5) + jj;
    qd[0] = (u16)pk; qd[8] = (u16)(pk >> 16);
    STAMP(3);
  } else if constexpr (ROLE == R_RED) {
    VMW(2); kk[5] = ldnt(kp + 5120);
    VMW(2); kk[6] = ldnt(kp + 6144);
  } else {
    VMW(1); kk[5] = ldnt(kp + 5120);
    VMW(1); kk[6] = ldnt(kp + 6144);
  }
  VMW(1); kk[7] = ldnt(kp + 7168);
  if constexpr (ROLE == R_HG) {
    const int hb = lane >> 5, fi = min(lane & 31, nf - 1), f = fs + fi, cb = f >> 6, m = (f & 63) >> 3;
    const int bg = 4 + cb, bu = 4 + (cb + 9 - UPROT) % 9;
    const int ng = (288 * bg + 287) / 117 - (288 * bg + 1) / 117 + 1, nu = (288 * bu + 287) / 117 - (288 * bu + 1) / 117 + 1;
    const u32 off = WS_P1 + (xc * 13 * 8 + hb) * 1024 + ((m & 3) >> 1) * 512 + (m >> 2) * 128 + (f & 7) * 16;
    u32x4 g[4], u[4];
#define LOADS_ _Pragma("unroll") for (int k = 0; k < 4; ++k) { g[k] = gload(rs, off + bg * 8192 + min(k, ng - 1) * 2048); u[k] = gload(rs, off + 256 + bu * 8192 + min(k, nu - 1) * 2048); }
    VMW(1);
    LOADS_
    vv[0] = ldnt(vp); vv[1] = ldnt(vp + 1024);
    CLOBBER();
    RETRY_ALL(LOADS_, gok(g[0], T) && gok(g[1], T) && gok(g[2], T) && gok(g[3], T) && gok(u[0], T) && gok(u[1], T) && gok(u[2], T) && gok(u[3], T), 8);
#undef LOADS_
    float g0 = __uint_as_float(g[0][0]) + __uint_as_float(g[1][0]) + __uint_as_float(g[2][0]), g2 = __uint_as_float(g[0][2]) + __uint_as_float(g[1][2]) + __uint_as_float(g[2][2]);
    float u0 = __uint_as_float(u[0][0]) + __uint_as_float(u[1][0]) + __uint_as_float(u[2][0]), u2 = __uint_as_float(u[0][2]) + __uint_as_float(u[1][2]) + __uint_as_float(u[2][2]);
    if (ng == 4) { g0 += __uint_as_float(g[3][0]); g2 += __uint_as_float(g[3][2]); }
    if (nu == 4) { u0 += __uint_as_float(u[3][0]); u2 += __uint_as_float(u[3][2]); }
    asm volatile("" : "+v"(g0), "+v"(g2), "+v"(u0), "+v"(u2));
    const float h = rbf((m & 1) ? g2 : g0) * rbf((m & 1) ? u2 : u0);
    if ((lane & 31) < nf) lds[L_H + (hb * 4 + (fi & 3)) * 8 + (fi >> 2)] = pkbf(h, h);
    STAMP(3);
  } else {
    VMW(1); vv[0] = ldnt(vp);
    VMW(1); vv[1] = ldnt(vp + 1024);
  }
  VMW(1); vv[2] = ldnt(vp + 2048);
  vv[3] = ldnt(vp + 3072);
  BAR();
  STAMP(4);
  u32 bb[8];
  float Mw = 0.f, Lw = 0.f;
  {
    const u32x4 q0 = *reinterpret_cast<const u32x4*>(lds + L_Q + 4 * c), q1 = *reinterpret_cast<const u32x4*>(lds + L_Q + 64 + 4 * c);
    float x[16];
#pragma unroll
    for (int i = 0; i < 8; ++i) {
      float d0 = 0.f, d1 = 0.f;
#pragma unroll
      for (int k = 0; k < 4; ++k) { d0 = dot2(kk[i][k], q0[k], d0); d1 = dot2(kk[i][k], q1[k], d1); }
      x[2 * i] = d0; x[2 * i + 1] = d1;
    }
#define TR_STEP(HALF, CTRL) _Pragma("unroll") for (int h = 0; h < (HALF); ++h) { \
      const float t1_ = x[h] + dpp<CTRL>(x[h]), t2_ = x[h + (HALF)] + dpp<CTRL>(x[h + (HALF)]); x[h] = (c & (HALF)) ? t2_ : t1_; }
    __builtin_amdgcn_sched_barrier(0);
    VMW(2); vv[4] = ldnt(vp + 4096);
    __builtin_amdgcn_sched_barrier(0);
    TR_STEP(8, ROR8) TR_STEP(4, XOR7) TR_STEP(2, XOR2) TR_STEP(1, XOR1)
#undef TR_STEP
    const float sc = rbf(rbf(x[0]) * 0.08838834764831845f);
    float m = fmaxf(sc, dpp<XOR2>(sc)); m = fmaxf(m, dpp<ROR4>(m)); m = fmaxf(m, dpp<ROR8>(m)); m = x32max(x16max(m));
    if (lane < 2) ldf[L_M + lane * 16 + wv] = m;
    VMW(2); vv[5] = ldnt(vp + 5120);
    BAR();
    STAMP(5);
    float M;
    {
      const f32x4* mp = reinterpret_cast<const f32x4*>(ldf + L_M + (c & 1) * 16);
      const f32x4 m0 = mp[0], m1 = mp[1], m2 = mp[2], m3 = mp[3];
      M = fmaxf(fmaxf(fmaxf(fmaxf(m0[0], m0[1]), fmaxf(m0[2], m0[3])), fmaxf(fmaxf(m1[0], m1[1]), fmaxf(m1[2], m1[3]))),
                fmaxf(fmaxf(fmaxf(m2[0], m2[1]), fmaxf(m2[2], m2[3])), fmaxf(fmaxf(m3[0], m3[1]), fmaxf(m3[2], m3[3]))));
    }
    const float p = __builtin_amdgcn_exp2f((sc - M) * 1.4426950408889634f);
    float l = p + dpp<XOR2>(p); l += dpp<ROR4>(l); l += dpp<ROR8>(l); l = x32sum(x16sum(l));
    if (lane < 2) ldf[L_L + lane * 16 + wv] = l;
    BAR();
    if constexpr (ROLE == R_ML) {
      Mw = M; Lw = ldf[L_L + (lane & 1) * 16 + (lane >> 1 & 15)];
      Lw += dpp<XOR2>(Lw); Lw += dpp<ROR4>(Lw); Lw += dpp<ROR8>(Lw); Lw = x16sum(Lw);
    }
    const int pb = (int)pkbf(p, p);
#define PBC(I) { int t_ = __builtin_amdgcn_update_dpp(0, pb, 0x150 + 2 * (I), 0xf, 0x3, false); bb[I] = (u32)__builtin_amdgcn_update_dpp(t_, pb, 0x151 + 2 * (I), 0xf, 0xc, false); }
    PBC(0) PBC(1) PBC(2) PBC(3) PBC(4) PBC(5) PBC(6) PBC(7)
#undef PBC
  }
  f32x4 acc = {0.f, 0.f, 0.f, 0.f};
#pragma unroll
  for (int i = 0; i < 4; ++i) acc = rowmma(vv[i], bb[i], mk, acc);
  __builtin_amdgcn_sched_barrier(0);
  if constexpr (ROLE == R_ML) {
    vv[6] = ldnt(vp + 6144); vv[7] = ldnt(vp + 7168);
    CLOBBER();
    gstore(rs, lane < 2 ? WS_ML + (kvh * 16 + jg) * 128 + lane * 16 : 0xFFFFFF00u, Mw, Lw, T);
    CLOBBER();
    acc = rowmma(vv[4], bb[4], mk, acc); acc = rowmma(vv[5], bb[5], mk, acc);
  } else {
#pragma unroll
    for (int i = 4; i < 6; ++i) { VMW(1); vv[i + 2] = ldnt(vp + (i + 2) * 1024); CLOBBER(); acc = rowmma(vv[i], bb[i], mk, acc); __builtin_amdgcn_sched_barrier(0); }
  }
  const int gam = 4 * w + ((lane >> 1) & 3);
  const int gl = gam < 512 ? (gam & 63) : (gam & 31), ghf = gam < 512 ? ((gam >> 6) & 1) : ((gam >> 5) & 1);
  const int oidx = ((gl >> 3) & 1) * DM + (gam < 512 ? 128 * (gam >> 7) : 512) + 32 * (gl >> 4) + 16 * ghf + (gl & 7) + 8 * (lane & 1);
  u32 xres = 0;
#define PV_TAIL() do { acc = rowmma(vv[6], bb[6], mk, acc); acc = rowmma(vv[7], bb[7], mk, acc); \
    *reinterpret_cast<f32x4*>(ldf + L_REC + wv * 256 + lane * 4) = acc; STAMP(6); BAR(); \
    const float* rp_ = ldf + L_REC + (lane >> 3) * 256 + (((wv >> 1) & 3) * 16 + (wv >> 3) * 8 + jj) * 4 + 2 * (wv & 1); \
    f32x2 s_ = *reinterpret_cast<const f32x2*>(rp_) + *reinterpret_cast<const f32x2*>(rp_ + 8 * 256); \
    s_[0] = x32sum(x16sum(s_[0] + dpp<ROR8>(s_[0]))); s_[1] = x32sum(x16sum(s_[1] + dpp<ROR8>(s_[1]))); \
    if (lane < 8) gstore(rs, WS_PO + ((kvh * 16 + wv) * 16 + jg) * 128 + jj * 16, s_[0], s_[1], T); STAMP(7); } while (0)
  if constexpr (ROLE == R_L2) {
    gchar* dp = (gchar*)cx.Wd + (size_t)(576 * xc + fs + r) * 1152 + 256 * (wv - 11) + 16 * c;
    gchar* op = (gchar*)cx.Wo + (size_t)(256 * kvh + 16 * jg + r) * 1152 + 256 * (wv - 11) + 16 * c;
    u32x4 ah[5], ao[4];
    VMW(1);
    ah[0] = ldnt(dp); ah[1] = ldnt(dp + 4608); ah[2] = ldnt(dp + 2 * 4608);
    CLOBBER();
    PV_TAIL();
    ah[3] = ldnt(dp + 3 * 4608);
    ah[4] = ldnt(dp + (nf == 20 ? 4 : 3) * 4608);
#pragma unroll
    for (int t = 0; t < 4; ++t) ao[t] = ldnt(op + t * 4608);
    CLOBBER();
    STAMP(8);
    const u32x4 hb = *reinterpret_cast<const u32x4*>(lds + L_H + (jb * 4 + r) * 8);
    const u32 hb4 = nf == 20 ? lds[L_H + (jb * 4 + r) * 8 + 4] : 0u;
    f32x4 accA = {0.f, 0.f, 0.f, 0.f};
#pragma unroll
    for (int t = 0; t < 4; ++t) accA = rowmma(ah[t], hb[t], mk, accA);
    accA = rowmma(ah[4], hb4, mk, accA);
    asm volatile("s_waitcnt vmcnt(0)" : "+v"(accA) :: "memory");
    STAMP(10);
    BAR();
    const u32x4 ob = *reinterpret_cast<const u32x4*>(lds + L_O + (jb * 4 + r) * 4);
#pragma unroll
    for (int t = 0; t < 4; ++t) accA = rowmma(ao[t], ob[t], mk, accA);
    const u32 off = WS_PF + w * 9216 + (wv - 11) * 2048 + lane * 16;
    gstore(rs, off, accA[0], accA[1], T);
    gstore(rs, off + 1024, accA[2], accA[3], T);
    STAMP(9);
  } else if constexpr (ROLE == R_L2H) {
    gchar* dp = (gchar*)cx.Wd + (size_t)(576 * xc + fs + r) * 1152 + 1024 + 16 * jj;
    gchar* op = (gchar*)cx.Wo + (size_t)(256 * kvh + 16 * jg + r + 4 * jb) * 1152 + 1024 + 16 * jj;
    u32x4 ah[3], ao[2];
    VMW(1);
    ah[0] = ldnt(dp + jb * 4608); ah[1] = ldnt(dp + jb * 4608 + 9216);
    CLOBBER();
    PV_TAIL();
    ah[2] = ldnt(dp + (nf == 20 ? 4 : 3) * 4608);
    ao[0] = ldnt(op); ao[1] = ldnt(op + 9216);
    CLOBBER();
    STAMP(8);
    const u32x4 hb = *reinterpret_cast<const u32x4*>(lds + L_H + (jb * 4 + r) * 8);
    const u32 hb4 = nf == 20 ? lds[L_H + (jb * 4 + r) * 8 + 4] : 0u;
    f32x4 accA = {0.f, 0.f, 0.f, 0.f}, accB = {0.f, 0.f, 0.f, 0.f};
    accA = rowmma(ah[0], hb[0], mk, accA); accB = rowmma(ah[0], hb[1], mk, accB);
    accA = rowmma(ah[1], hb[2], mk, accA); accB = rowmma(ah[1], hb[3], mk, accB);
    accA = rowmma(ah[2], hb4, mk, accA);
    asm volatile("s_waitcnt vmcnt(0)" : "+v"(accA), "+v"(accB) :: "memory");
    STAMP(10);
    BAR();
    const u32x4 ob = *reinterpret_cast<const u32x4*>(lds + L_O + (jb * 4 + r) * 4);
    accA = rowmma(ao[0], ob[0], mk, accA); accB = rowmma(ao[0], ob[1], mk, accB);
    accA = rowmma(ao[1], ob[2], mk, accA); accB = rowmma(ao[1], ob[3], mk, accB);
    float v[4];
#pragma unroll
    for (int k = 0; k < 4; ++k) v[k] = x32sum(lane < 32 ? accA[k] : accB[k]);
    gstore(rs, WS_PF + w * 9216 + 8192 + lane * 16, lane < 32 ? v[0] : v[2], lane < 32 ? v[1] : v[3], T);
    STAMP(9);
  } else {
    PV_TAIL();
    if constexpr (ROLE == R_HG) { if (w < 144) xres = cx.X[oidx]; }
    if constexpr (ROLE == R_ML) {
      const u32 oo = WS_PO + ((kvh * 16 + jg) * 16 + c) * 128 + r * 16, mo = WS_ML + (kvh * 16 + c) * 128 + (jg >> 3) * 16;
      u32x4 p0, p1, ml;
      __builtin_amdgcn_s_sleep(ODELAY);
      CLOBBER();
#define LOADS_ { p0 = gload(rs, oo); p1 = gload(rs, oo + 64); ml = gload(rs, mo); }
      LOADS_
      RETRY(LOADS_, gok(p0, T) && gok(p1, T) && gok(ml, T), PSLEEP);
#undef LOADS_
      STAMP(11);
      const float mm = __uint_as_float(ml[0]);
      float M = fmaxf(mm, dpp<XOR1>(mm)); M = fmaxf(M, dpp<XOR2>(M)); M = fmaxf(M, dpp<XOR7>(M));
      const float e = __builtin_amdgcn_exp2f((mm - M) * 1.4426950408889634f);
      float o[5] = {e * __uint_as_float(p0[0]), e * __uint_as_float(p1[0]), e * __uint_as_float(p0[2]), e * __uint_as_float(p1[2]), e * __uint_as_float(ml[2])};
#pragma unroll
      for (int t = 0; t < 5; ++t) { o[t] += dpp<XOR1>(o[t]); o[t] += dpp<XOR2>(o[t]); o[t] += dpp<XOR7>(o[t]); }
      const float inv = __builtin_amdgcn_rcpf(o[4]);
      u32x4 ob;
#pragma unroll
      for (int t = 0; t < 4; ++t) { const float y = o[t] * inv; ob[t] = pkbf(y, y); }
      if (jj == 0) *reinterpret_cast<u32x4*>(lds + L_O + (jb * 4 + r) * 4) = ob;
    }
    BAR();
  }
  if (w >= 144) { STAMP_DUMP(); return; }
  BAR();
  STAMP(12);
  __builtin_amdgcn_s_sleep(FDELAY);
  {
    const u32 off = WS_PF + (wv * 16 + (lane >> 2)) * 9216 + (4 * w + (lane & 3)) * 16;
    u32x4 g = gload(rs, off);
    RETRY(g = gload(rs, off), gok(g, T), FSLEEP);
    STAMP(13);
    float v0 = __uint_as_float(g[0]), v1 = __uint_as_float(g[2]);
    v0 += dpp<ROR4>(v0); v1 += dpp<ROR4>(v1); v0 += dpp<ROR8>(v0); v1 += dpp<ROR8>(v1);
    v0 = x32sum(x16sum(v0)); v1 = x32sum(x16sum(v1));
    if (lane < 4) *reinterpret_cast<float2*>(ldf + L_FIN + wv * 8 + lane * 2) = make_float2(v0, v1);
    BAR();
    if constexpr (ROLE == R_HG) {
      float s = ldf[L_FIN + (lane >> 3) * 16 + jj] + ldf[L_FIN + (lane >> 3) * 16 + 8 + jj];
      s = x32sum(x16sum(s + dpp<ROR8>(s)));
      if (lane < 8) cx.out[oidx] = (u16)pkbf(bf2f(xres) + s, 0.f);
      if (w == 0 && lane == 0) __builtin_amdgcn_raw_buffer_store_b32(T, rs, WS_E, 0, 16);
      STAMP(14);
    }
  }
  STAMP_DUMP();
}

__global__ void __launch_bounds__(1024, 4) fused_decode(const u16* __restrict__ X, const u16* __restrict__ Kc, const u16* __restrict__ Vc,
                                                        const u16* __restrict__ Wq, const u16* __restrict__ Wo, const u16* __restrict__ Wg,
                                                        const u16* __restrict__ Wu, const u16* __restrict__ Wd, u16* __restrict__ out, char* ws) {
  __shared__ __attribute__((aligned(16))) u32 lds[L_TOTAL];
  float* ldf = reinterpret_cast<float*>(lds);
  const int tid = threadIdx.x, lane = tid & 63, wv = __builtin_amdgcn_readfirstlane(tid >> 6);
  const int w = blockIdx.x, xc = w & 7, idx = w >> 3, kvh = 2 * xc + (idx >> 4), jg = idx & 15;
  const int r = lane >> 4, c = lane & 15, jb = (lane >> 3) & 1, jj = lane & 7;
  asm volatile("" :: "s"(X), "s"(Kc), "s"(Vc), "s"(Wq), "s"(Wo), "s"(Wg), "s"(Wu), "s"(Wd), "s"(out), "s"(ws));
  STAMP_INIT();
  STAMP(0);
  u32 xr[5];
  if (wv < 2) {
    const u32* xp = reinterpret_cast<const u32*>(X) + wv * 288 + lane;
#pragma unroll
    for (int t = 0; t < 4; ++t) xr[t] = xp[64 * t];
    xr[4] = xp[lane < 32 ? 256 : 0];
  }
  const int s0 = (117 * idx) >> 1, cnt = 58 + (idx & 1), bA = s0 / 144, sB = 144 * (bA + 1);
  const int t0 = s0 + ((cnt * wv) >> 4), t1 = s0 + ((cnt * (wv + 1)) >> 4);
  u32x4 a[4];
  {
    gchar* q0 = (gchar*)Wq + (r * 4096 + 512 * xc + 8 * c) * 2;
    gchar* g0 = (gchar*)(c < 8 ? Wg : Wu) + (r * FF + 576 * xc + 8 * (c & 7)) * 2;
    gchar* pA = bA < 4 ? q0 + 256 * bA : g0 + 128 * (c < 8 ? bA - 4 : (bA - 4 + UPROT) % 9);
    gchar* pB = bA < 3 ? q0 + 256 * (bA + 1) : g0 + 128 * (c < 8 ? bA - 3 : (bA - 3 + UPROT) % 9);
    const u32 strA = bA < 4 ? 32768 : 36864, strB = bA < 3 ? 32768 : 36864;
#pragma unroll
    for (int i = 0; i < 4; ++i) {
      const int sg = t0 + i;
      const u32 off = sg < sB ? (sg - 144 * bA) * strA : (sg - sB) * strB;
      gchar* p = (sg < sB ? pA : pB) + off;
      if (i < 3 || sg < t1) a[i] = ldnt(p);
    }
  }
  const size_t kvo = ((size_t)(((jg >> 3) * 16 + kvh) * SK + (jg & 7) * 512 + wv * 32)) * 256 + lane * 16;
  gchar* kp = (gchar*)Kc + kvo;
  const u32x4 k0 = ldnt(kp);
  CLOBBER();
  const u32 T = *reinterpret_cast<const __attribute__((address_space(4))) u32*>((uintptr_t)ws + WS_E) + 1u;
  const u32 mword = (jj & 1) ? 0xFFFF0000u : 0x0000FFFFu;
  const u32x4 mk = {(jj >> 1) == 0 ? mword : 0u, (jj >> 1) == 1 ? mword : 0u, (jj >> 1) == 2 ? mword : 0u, (jj >> 1) == 3 ? mword : 0u};
  if (wv < 2) {
    float v[10], s = 0.f;
#pragma unroll
    for (int t = 0; t < 5; ++t) { v[2 * t] = bf2f(xr[t] & 0xffffu); v[2 * t + 1] = __uint_as_float(xr[t] & 0xffff0000u); }
#pragma unroll
    for (int t = 0; t < 8; ++t) s += v[t];
    if (lane < 32) s += v[8] + v[9];
    const float mean = wavesum(s) * (1.f / DM);
    float q = 0.f;
#pragma unroll
    for (int t = 0; t < 8; ++t) { const float d = v[t] - mean; q = fmaf(d, d, q); }
    if (lane < 32) { const float d = v[8] - mean, e = v[9] - mean; q = fmaf(d, d, fmaf(e, e, q)); }
    const float mb = rbf(mean), den = rbf(__builtin_sqrtf(rbf(rbf(wavesum(q) * (1.f / DM)) + 1e-5f)));
    u32* xd = lds + L_XN + wv * 580 + 2 * lane;
#pragma unroll
    for (int t = 0; t < 5; ++t) {
      const float y0 = rbf(v[2 * t] - mb) / den, y1 = rbf(v[2 * t + 1] - mb) / den;
      uint2 o = {pkbf(y0, y0), pkbf(y1, y1)};
      if (t < 4 || lane < 32) *reinterpret_cast<uint2*>(xd + 128 * t) = o;
    }
  }
  BAR();
  STAMP(1);
  VMW(2);
  const u32x4 k1 = ldnt(kp + 1024);
  CLOBBER();
  {
    f32x4 acc = {0.f, 0.f, 0.f, 0.f};
    const u32* xb = lds + L_XN + jb * 580 + r;
#pragma unroll
    for (int i = 0; i < 4; ++i) {
      const int sg = t0 + i;
      if (i > 0 && sg == sB && sg < t1) { *reinterpret_cast<f32x4*>(ldf + L_REC + 16 * 256 + lane * 4) = acc; acc = f32x4{0.f, 0.f, 0.f, 0.f}; }
      if (i < 3 || sg < t1) acc = rowmma(a[i], xb[4 * (sg < sB ? sg - 144 * bA : sg - sB)], mk, acc);
    }
    *reinterpret_cast<f32x4*>(ldf + L_REC + wv * 256 + lane * 4) = acc;
  }
  const u32x4 k2 = ldnt(kp + 2048);
  BAR();
  STAMP(2);
  Ctx cx;
  cx.X = X; cx.Wo = Wo; cx.Wd = Wd; cx.out = out; cx.ws = ws; cx.kp = kp; cx.vp = (gchar*)Vc + kvo; cx.lds = lds;
  cx.rs = __builtin_amdgcn_make_buffer_rsrc((void*)ws, 0, WS_END, 0x00020000); cx.mk = mk; cx.T = T;
  cx.lane = lane; cx.wv = wv; cx.w = w; cx.s0 = s0; cx.cnt = cnt; cx.bA = bA; cx.sB = sB;
  if (wv < 4) rest<R_RED>(cx, k0, k1, k2);
  else if (wv < 6) rest<R_QG>(cx, k0, k1, k2);
  else if (wv == 6) rest<R_HG>(cx, k0, k1, k2);
  else if (wv == 7) rest<R_ML>(cx, k0, k1, k2);
  else if (wv < 11) rest<R_PLAIN>(cx, k0, k1, k2);
  else if (wv < 15) rest<R_L2>(cx, k0, k1, k2);
  else rest<R_L2H>(cx, k0, k1, k2);
}

extern "C" void kernel_launch(void* const* d_in, const int* in_sizes, int n_in, void* d_out, int out_size, void* d_ws, size_t ws_size,
                              hipStream_t stream) {
  fused_decode<<<256, 1024, 0, stream>>>((const u16*)d_in[0], (const u16*)d_in[1], (const u16*)d_in[2], (const u16*)d_in[3], (const u16*)d_in[4],
                                         (const u16*)d_in[6], (const u16*)d_in[7], (const u16*)d_in[8], (u16*)d_out, (char*)d_ws);
}
```

```cpp
#include <hip/hip_runtime.h>
#include <hip/hip_bf16.h>
#include <stdint.h>

typedef unsigned short u16;
typedef unsigned int u32;
typedef unsigned long long u64;
using u32x4 = __attribute__((ext_vector_type(4))) unsigned;
using u32x3 = __attribute__((ext_vector_type(3))) unsigned;
using f32x4 = __attribute__((ext_vector_type(4))) float;
using f32x2 = __attribute__((ext_vector_type(2))) float;
using bf16x8 = __attribute__((ext_vector_type(8))) __bf16;
using bf16x2 = __attribute__((ext_vector_type(2))) __bf16;
typedef __attribute__((address_space(1))) const u32x4 gvec;
typedef __attribute__((address_space(1))) const char gchar;

constexpr int DM = 576, SK = 4096, FF = 4608;
constexpr u32 WS_E = 0;
constexpr u32 WS_ML = 4096;
constexpr u32 WS_PO = 65536;
constexpr u32 WS_P1 = WS_PO + 524288;
constexpr u32 WS_PF = WS_P1 + 8 * 13 * 4 * 2048;
constexpr u32 WS_END = WS_PF + 256 * 9216;
constexpr u32 SPIN_CAP = 1u << 20;

constexpr int L_XN = 0;
constexpr int L_CNT = 1160;
constexpr int L_Q = 1168;
constexpr int L_H = 1296;
constexpr int L_O = 1360;
constexpr int L_M = 1392;
constexpr int L_L = 1424;
constexpr int L_FIN = 1456;
constexpr int L_P1 = 1600;
constexpr int L_REC = L_P1 + 4096;
constexpr int L_ST = L_REC + 4096;
enum { C_P1 = 0, C_Q = 1, C_M = 2, C_L = 3, C_X = 4 };
constexpr int L_TOTAL = L_ST;
#define STAMP(id)
#define STAMP_INIT()
#define STAMP_DUMP()
#define FSLEEP 4
#define PSLEEP 2
#define ODELAY 16
#define FDELAY 14
#define PFMODE 1
#define WQ 1
#define UPROT 4
#define WMID 2

__device__ __forceinline__ float bf2f(u32 x) { return __uint_as_float(x << 16); }
__device__ __forceinline__ float rbf(float x) { return (float)(__bf16)x; }
__device__ __forceinline__ u32 pkbf(float lo, float hi) { bf16x2 v = {(__bf16)lo, (__bf16)hi}; return __builtin_bit_cast(u32, v); }
__device__ __forceinline__ u32x4 ldnt(gchar* p) { return __builtin_nontemporal_load((gvec*)p); }
__device__ __forceinline__ float dot2(u32 a, u32 b, float c) {
  return __builtin_amdgcn_fdot2_f32_bf16(__builtin_bit_cast(bf16x2, a), __builtin_bit_cast(bf16x2, b), c, false);
}
template <int CTRL> __device__ __forceinline__ float dpp(float x) {
  return __builtin_bit_cast(float, __builtin_amdgcn_mov_dpp(__builtin_bit_cast(int, x), CTRL, 0xf, 0xf, true));
}
constexpr int XOR1 = 0xB1, XOR2 = 0x4E, XOR7 = 0x141, ROR8 = 0x128, ROR4 = 0x124;
__device__ __forceinline__ float x16sum(float x) {
  auto s = __builtin_amdgcn_permlane16_swap(__float_as_uint(x), __float_as_uint(x), false, false);
  return __uint_as_float(s[0]) + __uint_as_float(s[1]);
}
__device__ __forceinline__ float x32sum(float x) {
  auto s = __builtin_amdgcn_permlane32_swap(__float_as_uint(x), __float_as_uint(x), false, false);
  return __uint_as_float(s[0]) + __uint_as_float(s[1]);
}
__device__ __forceinline__ float x16max(float x) {
  auto s = __builtin_amdgcn_permlane16_swap(__float_as_uint(x), __float_as_uint(x), false, false);
  return fmaxf(__uint_as_float(s[0]), __uint_as_float(s[1]));
}
__device__ __forceinline__ float x32max(float x) {
  auto s = __builtin_amdgcn_permlane32_swap(__float_as_uint(x), __float_as_uint(x), false, false);
  return fmaxf(__uint_as_float(s[0]), __uint_as_float(s[1]));
}
__device__ __forceinline__ float wavesum(float s) {
  s += dpp<XOR1>(s); s += dpp<XOR2>(s); s += dpp<XOR7>(s); s += dpp<ROR8>(s); return x32sum(x16sum(s));
}
__device__ __forceinline__ f32x4 rowmma(u32x4 a, u32 bb, u32x4 mk, f32x4 acc) {
  u32x4 b = {bb & mk[0], bb & mk[1], bb & mk[2], bb & mk[3]};
  return __builtin_amdgcn_mfma_f32_16x16x32_bf16(__builtin_bit_cast(bf16x8, a), __builtin_bit_cast(bf16x8, b), acc, 0, 0, 0);
}
#define CLOBBER() asm volatile("" ::: "memory")
#define BAR() do { asm volatile("s_waitcnt lgkmcnt(0)" ::: "memory"); __builtin_amdgcn_s_barrier(); asm volatile("" ::: "memory"); } while (0)
#define VMW_(n) asm volatile("s_waitcnt vmcnt(" #n ")" ::: "memory")
#define VMW(n) VMW_(n)
#define RETRY(LOADS, OK, SL) do { if (!__all(OK)) { _Pragma("unroll 1") for (u32 spins = 0; spins < SPIN_CAP; ++spins) { \
      __builtin_amdgcn_s_sleep(SL); CLOBBER(); if (!(OK)) { LOADS; } if (__all(OK)) break; } } } while (0)
#define RETRY_ALL(LOADS, OK, SL) do { if (!__all(OK)) { _Pragma("unroll 1") for (u32 spins = 0; spins < SPIN_CAP; ++spins) { \
      __builtin_amdgcn_s_sleep(SL); CLOBBER(); LOADS; if (__all(OK)) break; } } } while (0)
__device__ __forceinline__ void gstore(__amdgpu_buffer_rsrc_t r, u32 off, float a, float b, u32 T) {
  u32x4 v = {__float_as_uint(a), T, __float_as_uint(b), T};
  __builtin_amdgcn_raw_buffer_store_b128(v, r, off, 0, 16);
}
__device__ __forceinline__ u32x4 gload(__amdgpu_buffer_rsrc_t r, u32 off) { return __builtin_amdgcn_raw_buffer_load_b128(r, off, 0, 16); }
__device__ __forceinline__ bool gok(u32x4 v, u32 T) { return v[1] == T && v[3] == T; }

__device__ __forceinline__ void arrive(u32* p, int lane) {
  CLOBBER();
  if (lane == 0) __hip_atomic_fetch_add(p, 1u, __ATOMIC_RELAXED, __HIP_MEMORY_SCOPE_WORKGROUP);
  CLOBBER();
}
__device__ __forceinline__ void await(u32* p, u32 n) {
  _Pragma("unroll 1") for (u32 spins = 0; spins < SPIN_CAP; ++spins) {
    if (__builtin_amdgcn_readfirstlane(__hip_atomic_load(p, __ATOMIC_RELAXED, __HIP_MEMORY_SCOPE_WORKGROUP)) >= n) break;
    __builtin_amdgcn_s_sleep(1);
  }
  CLOBBER();
}
#define SB() __builtin_amdgcn_sched_barrier(0)

struct Ctx {
  const u16* X; const u16* Wo; const u16* Wd; u16* out; char* ws; gchar* kp; gchar* vp; u32* lds;
  __amdgpu_buffer_rsrc_t rs; u32x4 mk; u32 T;
  int lane, wv, w, bA, wA, hasB;
};
enum { R_RED, R_QG, R_HG, R_ML, R_PLAIN, R_L2, R_L2H };

template <int ROLE>
__device__ __forceinline__ void rest(const Ctx& cx, u32x4 k0, u32x4 k1) {
  u32* lds = cx.lds; float* ldf = reinterpret_cast<float*>(lds);
  const int lane = cx.lane, wv = cx.wv, w = cx.w, xc = w & 7, idx = w >> 3, kvh = 2 * xc + (idx >> 4), jg = idx & 15;
  const int r = lane >> 4, c = lane & 15, jb = (lane >> 3) & 1, jj = lane & 7;
  const int fs = 18 * idx + ((idx & 1) << 1), nf = (idx & 1) ? 16 : 20;
  const auto rs = cx.rs; const u32x4 mk = cx.mk; const u32 T = cx.T; char* ws = cx.ws;
  gchar* kp = cx.kp; gchar* vp = cx.vp;
  u32x4 kk[8], vv[8];
  kk[0] = k0; kk[1] = k1;
  if constexpr (ROLE == R_RED) {
    kk[2] = ldnt(kp + 2048); kk[3] = ldnt(kp + 3072);
    const int sgm = wv >> 1, hf = wv & 1, beta = cx.bA + sgm;
    const u32 off = WS_P1 + (((xc * 13 + beta) * 4 + idx - (288 * beta + 1) / 117) * 2 + jb) * 1024 + hf * 512 + r * 128 + jj * 16;
    const u32 sel = sgm ? (0xFFFFu << cx.wA) : ~(0xFFFFu << cx.wA);
    await(lds + L_CNT + C_P1, 16);
    const float* rp = ldf + L_P1 + lane * 4 + 2 * hf;
    f32x2 v[16], s = {0.f, 0.f};
#pragma unroll
    for (int k = 0; k < 16; ++k) v[k] = *reinterpret_cast<const f32x2*>(rp + k * 256);
#pragma unroll
    for (int k = 0; k < 16; ++k) { const float wk = (float)((sel >> k) & 1u); s[0] = fmaf(wk, v[k][0], s[0]); s[1] = fmaf(wk, v[k][1], s[1]); }
    gstore(rs, sgm == 0 || cx.hasB ? off : 0xFFFFFF00u, s[0], s[1], T);
    STAMP(3);
    VMW(2); kk[4] = ldnt(kp + 4096);
    VMW(2); kk[5] = ldnt(kp + 5120);
    VMW(2); kk[6] = ldnt(kp + 6144);
    VMW(1); kk[7] = ldnt(kp + 7168);
  } else {
    VMW(1); kk[2] = ldnt(kp + 2048);
    VMW(1); kk[3] = ldnt(kp + 3072);
    VMW(1); kk[4] = ldnt(kp + 4096);
    VMW(1); kk[5] = ldnt(kp + 5120);
    if constexpr (ROLE == R_QG) {
      const int beta = 2 * (idx >> 4) + wv - 4, ns = (288 * beta + 287) / 117 - (288 * beta + 1) / 117 + 1;
      const u32 off = WS_P1 + ((xc * 13 + beta) * 8 + (jg >> 3)) * 1024 + lane * 16;
      u32x4 g[4];
      VMW(1);
#define LOADS_ _Pragma("unroll") for (int k = 0; k < 4; ++k) g[k] = gload(rs, off + min(k, ns - 1) * 2048);
      LOADS_
      kk[6] = ldnt(kp + 6144); kk[7] = ldnt(kp + 7168);
      CLOBBER();
      RETRY_ALL(LOADS_, gok(g[0], T) && gok(g[1], T) && gok(g[2], T) && gok(g[3], T), 8);
#undef LOADS_
      float v0 = __uint_as_float(g[0][0]) + __uint_as_float(g[1][0]) + __uint_as_float(g[2][0]);
      float v1 = __uint_as_float(g[0][2]) + __uint_as_float(g[1][2]) + __uint_as_float(g[2][2]);
      if (ns == 4) { v0 += __uint_as_float(g[3][0]); v1 += __uint_as_float(g[3][2]); }
      const u32 pk = pkbf(v0, v1);
      u16* qd = reinterpret_cast<u16*>(lds + L_Q) + (wv - 4) * 128 + 32 * ((lane >> 3) & 3) + 16 * (lane >> 5) + jj;
      qd[0] = (u16)pk; qd[8] = (u16)(pk >> 16);
      arrive(lds + L_CNT + C_Q, lane);
      STAMP(3);
    } else {
      VMW(1); kk[6] = ldnt(kp + 6144);
      VMW(1); kk[7] = ldnt(kp + 7168);
    }
  }
  VMW(1); vv[0] = ldnt(vp);
  VMW(1); vv[1] = ldnt(vp + 1024);
  if (WQ == 1) VMW(1);
  vv[2] = ldnt(vp + 2048);
  if (WQ == 2) { VMW(2); vv[3] = ldnt(vp + 3072); }
  CLOBBER();
  await(lds + L_CNT + C_Q, 2);
  STAMP(4);
  float x[16];
  {
    const u32x4 q0 = *reinterpret_cast<const u32x4*>(lds + L_Q + 4 * c), q1 = *reinterpret_cast<const u32x4*>(lds + L_Q + 64 + 4 * c);
#pragma unroll
    for (int i = 0; i < 8; ++i) {
      if (i == 5) { SB(); VMW(WQ); vv[2 + WQ] = ldnt(vp + (2 + WQ) * 1024); SB(); }
      float d0 = 0.f, d1 = 0.f;
#pragma unroll
      for (int k = 0; k < 4; ++k) { d0 = dot2(kk[i][k], q0[k], d0); d1 = dot2(kk[i][k], q1[k], d1); }
      x[2 * i] = d0; x[2 * i + 1] = d1;
    }
  }
#define TR_STEP(HALF, CTRL) _Pragma("unroll") for (int h = 0; h < (HALF); ++h) { \
    const float t1_ = x[h] + dpp<CTRL>(x[h]), t2_ = x[h + (HALF)] + dpp<CTRL>(x[h + (HALF)]); x[h] = (c & (HALF)) ? t2_ : t1_; }
  TR_STEP(8, ROR8)
  u32x4 g[4], u[4]; u32 hoff = 0; int bg = 0, bu = 0, ng = 0, nu = 0;
#define HLOADS_ _Pragma("unroll") for (int k = 0; k < 4; ++k) { g[k] = gload(rs, hoff + bg * 8192 + min(k, ng - 1) * 2048); u[k] = gload(rs, hoff + 256 + bu * 8192 + min(k, nu - 1) * 2048); }
  const int fi = min(lane & 31, nf - 1), ff = fs + fi;
  if constexpr (ROLE == R_HG) {
    const int cb = ff >> 6, m = (ff & 63) >> 3;
    bg = 4 + cb; bu = 4 + (cb + 9 - UPROT) % 9;
    ng = (288 * bg + 287) / 117 - (288 * bg + 1) / 117 + 1; nu = (288 * bu + 287) / 117 - (288 * bu + 1) / 117 + 1;
    hoff = WS_P1 + (xc * 13 * 8 + (lane >> 5)) * 1024 + ((m & 3) >> 1) * 512 + (m >> 2) * 128 + (ff & 7) * 16;
    SB(); VMW(WQ);
    HLOADS_
    vv[3 + WQ] = ldnt(vp + (3 + WQ) * 1024);
    SB();
  } else { SB(); VMW(WQ); vv[3 + WQ] = ldnt(vp + (3 + WQ) * 1024); SB(); }
  TR_STEP(4, XOR7) TR_STEP(2, XOR2) TR_STEP(1, XOR1)
#undef TR_STEP
  u32 bb[8];
  {
    const float sc = rbf(rbf(x[0]) * 0.08838834764831845f);
    float m = fmaxf(sc, dpp<XOR2>(sc)); m = fmaxf(m, dpp<ROR4>(m)); m = fmaxf(m, dpp<ROR8>(m)); m = x32max(x16max(m));
    const float p = __builtin_amdgcn_exp2f((sc - m) * 1.4426950408889634f);
    float l = p + dpp<XOR2>(p); l += dpp<ROR4>(l); l += dpp<ROR8>(l); l = x32sum(x16sum(l));
    if (lane < 2) { ldf[L_M + lane * 16 + wv] = m; ldf[L_L + lane * 16 + wv] = l; }
    arrive(lds + L_CNT + C_M, lane);
    STAMP(5);
    if (WQ == 1) { if constexpr (ROLE == R_HG) { vv[5] = ldnt(vp + 5120); CLOBBER(); } else { SB(); VMW(1); vv[5] = ldnt(vp + 5120); SB(); } }
    const int pb = (int)pkbf(p, p);
#define PBC(I) { int t_ = __builtin_amdgcn_update_dpp(0, pb, 0x150 + 2 * (I), 0xf, 0x3, false); bb[I] = (u32)__builtin_amdgcn_update_dpp(t_, pb, 0x151 + 2 * (I), 0xf, 0xc, false); }
    PBC(0) PBC(1) PBC(2) PBC(3) PBC(4) PBC(5) PBC(6) PBC(7)
#undef PBC
  }
  f32x4 acc = {0.f, 0.f, 0.f, 0.f};
#pragma unroll
  for (int i = 0; i < 4; ++i) acc = rowmma(vv[i], bb[i], mk, acc);
  SB();
  if constexpr (ROLE == R_HG) {
    RETRY_ALL(HLOADS_, gok(g[0], T) && gok(g[1], T) && gok(g[2], T) && gok(g[3], T) && gok(u[0], T) && gok(u[1], T) && gok(u[2], T) && gok(u[3], T), 8);
    vv[6] = ldnt(vp + 6144);
    float g0 = __uint_as_float(g[0][0]) + __uint_as_float(g[1][0]) + __uint_as_float(g[2][0]), g2 = __uint_as_float(g[0][2]) + __uint_as_float(g[1][2]) + __uint_as_float(g[2][2]);
    float u0 = __uint_as_float(u[0][0]) + __uint_as_float(u[1][0]) + __uint_as_float(u[2][0]), u2 = __uint_as_float(u[0][2]) + __uint_as_float(u[1][2]) + __uint_as_float(u[2][2]);
    if (ng == 4) { g0 += __uint_as_float(g[3][0]); g2 += __uint_as_float(g[3][2]); }
    if (nu == 4) { u0 += __uint_as_float(u[3][0]); u2 += __uint_as_float(u[3][2]); }
    asm volatile("" : "+v"(g0), "+v"(g2), "+v"(u0), "+v"(u2));
    const float h = rbf((ff & 8) ? g2 : g0) * rbf((ff & 8) ? u2 : u0);
    if ((lane & 31) < nf) lds[L_H + ((lane >> 5) * 4 + (fi & 3)) * 8 + (fi >> 2)] = pkbf(h, h);
    STAMP(3);
    VMW(2); vv[7] = ldnt(vp + 7168); CLOBBER();
    acc = rowmma(vv[4], bb[4], mk, acc); acc = rowmma(vv[5], bb[5], mk, acc);
  } else {
#pragma unroll
    for (int i = 4; i < 6; ++i) { VMW(1); vv[i + 2] = ldnt(vp + (i + 2) * 1024); CLOBBER(); acc = rowmma(vv[i], bb[i], mk, acc); SB(); }
  }
#undef HLOADS_
  f32x4 fw = {0.f, 0.f, 0.f, 0.f};
  if constexpr (ROLE == R_ML) {
    await(lds + L_CNT + C_M, 16);
    const float mw = ldf[L_M + (lane & 31)];
    float M = fmaxf(mw, dpp<XOR1>(mw)); M = fmaxf(M, dpp<XOR2>(M)); M = fmaxf(M, dpp<XOR7>(M)); M = fmaxf(M, dpp<ROR8>(M));
    float L = ldf[L_L + (lane & 31)] * __builtin_amdgcn_exp2f((mw - M) * 1.4426950408889634f);
    L += dpp<XOR1>(L); L += dpp<XOR2>(L); L += dpp<XOR7>(L); L += dpp<ROR8>(L);
    gstore(rs, lane < 32 && c == 0 ? WS_ML + (kvh * 16 + jg) * 128 + r * 16 : 0xFFFFFF00u, M, L, T);
    CLOBBER();
  } else if constexpr (ROLE == R_RED) {
    await(lds + L_CNT + C_M, 16);
    const f32x4 m4 = *reinterpret_cast<const f32x4*>(ldf + L_M + jb * 16 + 4 * r);
    const float M = x32max(x16max(fmaxf(fmaxf(m4[0], m4[1]), fmaxf(m4[2], m4[3]))));
#pragma unroll
    for (int k = 0; k < 4; ++k) fw[k] = __builtin_amdgcn_exp2f((m4[k] - M) * 1.4426950408889634f);
    asm volatile("" : "+v"(fw));
  }
  constexpr int NCONS = PFMODE == 2 ? 36 : 72, PFB = PFMODE == 2 ? 4608 : 9216;
  const u32 pfo = WS_PF + (kvh * 16 + jg) * PFB;
  int oidx; u32 xres = 0;
  if (PFMODE == 2) {
    const int gam = 8 * w + ((lane >> 2) & 7), gl = gam < 256 ? (gam & 63) : (gam & 31);
    oidx = ((gl >> 3) & 1) * DM + (gam < 256 ? 128 * (gam >> 6) : 512) + 32 * (gl >> 4) + (gl & 7) + 8 * (lane & 3);
  } else {
    const int gam = 8 * w + ((lane >> 1) & 7), gl = gam < 512 ? (gam & 63) : (gam & 31), ghf = gam < 512 ? ((gam >> 6) & 1) : ((gam >> 5) & 1);
    oidx = ((gl >> 3) & 1) * DM + (gam < 512 ? 128 * (gam >> 7) : 512) + 32 * (gl >> 4) + 16 * ghf + (gl & 7) + 8 * (lane & 1);
  }
#define PV_TAIL() do { acc = rowmma(vv[6], bb[6], mk, acc); acc = rowmma(vv[7], bb[7], mk, acc); \
    *reinterpret_cast<f32x4*>(ldf + L_REC + wv * 256 + lane * 4) = acc; STAMP(6); BAR(); } while (0)
  if constexpr (ROLE == R_L2) {
    gchar* dp = (gchar*)cx.Wd + (size_t)(576 * xc + fs + r) * 1152 + 256 * (wv - 11) + 16 * c;
    gchar* op = (gchar*)cx.Wo + (size_t)(256 * kvh + 16 * jg + r) * 1152 + 256 * (wv - 11) + 16 * c;
    u32x4 ah[5], ao[4];
    VMW(1);
#pragma unroll
    for (int t = 0; t < 4; ++t) ah[t] = ldnt(dp + t * 4608);
    ah[4] = ldnt(dp + (nf == 20 ? 4 : 3) * 4608);
    CLOBBER();
    PV_TAIL();
#pragma unroll
    for (int t = 0; t < 4; ++t) ao[t] = ldnt(op + t * 4608);
    CLOBBER();
    STAMP(8);
    const u32x4 hb = *reinterpret_cast<const u32x4*>(lds + L_H + (jb * 4 + r) * 8);
    const u32 hb4 = nf == 20 ? lds[L_H + (jb * 4 + r) * 8 + 4] : 0u;
    f32x4 accA = {0.f, 0.f, 0.f, 0.f};
#pragma unroll
    for (int t = 0; t < 4; ++t) accA = rowmma(ah[t], hb[t], mk, accA);
    accA = rowmma(ah[4], hb4, mk, accA);
    u32 off = pfo + (wv - 11) * (PFMODE == 2 ? 1024 : 2048) + lane * 16, oa = (L_O + (jb * 4 + r) * 4) * 4;
    asm volatile("s_waitcnt vmcnt(0)" : "+v"(accA), "+v"(off), "+v"(oa) :: "memory");
    STAMP(10);
    BAR();
    const u32x4 ob = *reinterpret_cast<const u32x4*>(reinterpret_cast<const char*>(lds) + oa);
#pragma unroll
    for (int t = 0; t < 4; ++t) accA = rowmma(ao[t], ob[t], mk, accA);
    if (PFMODE == 2) gstore(rs, off, __uint_as_float(pkbf(accA[0], accA[1])), __uint_as_float(pkbf(accA[2], accA[3])), T);
    else { gstore(rs, off, accA[0], accA[1], T); gstore(rs, off + 1024, accA[2], accA[3], T); }
    STAMP(9);
  } else if constexpr (ROLE == R_L2H) {
    gchar* dp = (gchar*)cx.Wd + (size_t)(576 * xc + fs + r) * 1152 + 1024 + 16 * jj;
    gchar* op = (gchar*)cx.Wo + (size_t)(256 * kvh + 16 * jg + r + 4 * jb) * 1152 + 1024 + 16 * jj;
    u32x4 ah[3], ao[2];
    VMW(1);
    ah[0] = ldnt(dp + jb * 4608); ah[1] = ldnt(dp + jb * 4608 + 9216);
    ah[2] = ldnt(dp + (nf == 20 ? 4 : 3) * 4608);
    CLOBBER();
    PV_TAIL();
    ao[0] = ldnt(op); ao[1] = ldnt(op + 9216);
    CLOBBER();
    STAMP(8);
    const u32x4 hb = *reinterpret_cast<const u32x4*>(lds + L_H + (jb * 4 + r) * 8);
    const u32 hb4 = nf == 20 ? lds[L_H + (jb * 4 + r) * 8 + 4] : 0u;
    f32x4 accA = {0.f, 0.f, 0.f, 0.f}, accB = {0.f, 0.f, 0.f, 0.f};
    accA = rowmma(ah[0], hb[0], mk, accA); accB = rowmma(ah[0], hb[1], mk, accB);
    accA = rowmma(ah[1], hb[2], mk, accA); accB = rowmma(ah[1], hb[3], mk, accB);
    accA = rowmma(ah[2], hb4, mk, accA);
    u32 off = PFMODE == 2 ? (lane < 32 ? pfo + 4096 + lane * 16 : 0xFFFFFF00u) : pfo + 8192 + lane * 16, oa = (L_O + (jb * 4 + r) * 4) * 4;
    asm volatile("s_waitcnt vmcnt(0)" : "+v"(accA), "+v"(accB), "+v"(off), "+v"(oa) :: "memory");
    STAMP(10);
    BAR();
    const u32x4 ob = *reinterpret_cast<const u32x4*>(reinterpret_cast<const char*>(lds) + oa);
    accA = rowmma(ao[0], ob[0], mk, accA); accB = rowmma(ao[0], ob[1], mk, accB);
    accA = rowmma(ao[1], ob[2], mk, accA); accB = rowmma(ao[1], ob[3], mk, accB);
    if (PFMODE == 2) {
      float v[4];
#pragma unroll
      for (int k = 0; k < 4; ++k) { auto t = __builtin_amdgcn_permlane32_swap(__float_as_uint(accB[k]), __float_as_uint(accB[k]), false, false); v[k] = accA[k] + __uint_as_float(t[1]); }
      gstore(rs, off, __uint_as_float(pkbf(v[0], v[1])), __uint_as_float(pkbf(v[2], v[3])), T);
    } else {
      float v[2];
#pragma unroll
      for (int k = 0; k < 2; ++k) {
        auto t = __builtin_amdgcn_permlane32_swap(__float_as_uint(accB[k]), __float_as_uint(accA[k + 2]), false, false);
        v[k] = lane < 32 ? accA[k] + __uint_as_float(t[1]) : __uint_as_float(t[0]) + accB[k + 2];
      }
      gstore(rs, off, v[0], v[1], T);
    }
    STAMP(9);
  } else if constexpr (ROLE == R_RED) {
    u32 off = lane < 32 ? WS_PO + ((kvh * 16 + 8 * jb + 2 * wv + r) * 16 + jg) * 128 + jj * 16 : 0xFFFFFF00u, ra = (L_REC + r * 1024 + (wv * 16 + c) * 4) * 4;
    asm volatile("" : "+v"(off), "+v"(ra));
    PV_TAIL();
    const f32x4* rp = reinterpret_cast<const f32x4*>(reinterpret_cast<const char*>(lds) + ra);
    const f32x4 s = (fw[0] * rp[0] + fw[1] * rp[64]) + (fw[2] * rp[128] + fw[3] * rp[192]);
    const auto t0 = __builtin_amdgcn_permlane16_swap(__float_as_uint(s[0]), __float_as_uint(s[2]), false, false);
    const auto t1 = __builtin_amdgcn_permlane16_swap(__float_as_uint(s[1]), __float_as_uint(s[3]), false, false);
    gstore(rs, off, x32sum(__uint_as_float(t0[0]) + __uint_as_float(t0[1])), x32sum(__uint_as_float(t1[0]) + __uint_as_float(t1[1])), T);
    STAMP(7);
    BAR();
  } else if constexpr (ROLE == R_ML) {
    const u32 oo = WS_PO + ((kvh * 16 + jg) * 16 + c) * 128 + r * 16, mo = WS_ML + (kvh * 16 + c) * 128 + (jg >> 3) * 16;
    PV_TAIL();
    u32x4 p0, p1, ml = gload(rs, mo);
    RETRY(ml = gload(rs, mo), gok(ml, T), PSLEEP);
    const float mm = __uint_as_float(ml[0]);
    float Mx = fmaxf(mm, dpp<XOR1>(mm)); Mx = fmaxf(Mx, dpp<XOR2>(Mx)); Mx = fmaxf(Mx, dpp<XOR7>(Mx));
    const float e = __builtin_amdgcn_exp2f((mm - Mx) * 1.4426950408889634f);
    float L = e * __uint_as_float(ml[2]); L += dpp<XOR1>(L); L += dpp<XOR2>(L); L += dpp<XOR7>(L);
    float wt = e / L; u32 oa = (L_O + (jb * 4 + r) * 4) * 4;
    asm volatile("" : "+v"(wt), "+v"(oa));
    __builtin_amdgcn_s_sleep(ODELAY);
    CLOBBER();
#define LOADS_ { p0 = gload(rs, oo); p1 = gload(rs, oo + 64); }
    LOADS_
    RETRY(LOADS_, gok(p0, T) && gok(p1, T), PSLEEP);
#undef LOADS_
    STAMP(11);
    float o[4] = {wt * __uint_as_float(p0[0]), wt * __uint_as_float(p1[0]), wt * __uint_as_float(p0[2]), wt * __uint_as_float(p1[2])};
    u32x4 ob;
#pragma unroll
    for (int t = 0; t < 4; ++t) { o[t] += dpp<XOR1>(o[t]); o[t] += dpp<XOR2>(o[t]); o[t] += dpp<XOR7>(o[t]); ob[t] = pkbf(o[t], o[t]); }
    *reinterpret_cast<u32x4*>(reinterpret_cast<char*>(lds) + oa) = ob;
    BAR();
  } else {
    PV_TAIL();
    if constexpr (ROLE == R_HG) { if (w < NCONS) xres = cx.X[oidx]; }
    BAR();
  }
  if (w >= NCONS) { STAMP_DUMP(); return; }
  u32 foff = WS_PF + (wv * 16 + (lane >> 3)) * PFB + w * 128 + jj * 16;
  asm volatile("" : "+v"(foff));
  BAR();
  STAMP(12);
  __builtin_amdgcn_s_sleep(FDELAY);
  {
    u32x4 g0 = gload(rs, foff), g1 = gload(rs, foff + 8 * PFB);
    if (!__all(gok(g0, T) && gok(g1, T))) {
      _Pragma("unroll 1") for (u32 spins = 0; spins < SPIN_CAP; ++spins) {
        __builtin_amdgcn_s_sleep(FSLEEP); CLOBBER();
        if (!gok(g0, T)) g0 = gload(rs, foff);
        if (!gok(g1, T)) g1 = gload(rs, foff + 8 * PFB);
        if (__all(gok(g0, T) && gok(g1, T))) break;
      }
    }
    STAMP(13);
    if (PFMODE == 2) {
      float v[4] = {bf2f(g0[0] & 0xffffu) + bf2f(g1[0] & 0xffffu), __uint_as_float(g0[0] & 0xffff0000u) + __uint_as_float(g1[0] & 0xffff0000u),
                    bf2f(g0[2] & 0xffffu) + bf2f(g1[2] & 0xffffu), __uint_as_float(g0[2] & 0xffff0000u) + __uint_as_float(g1[2] & 0xffff0000u)};
#pragma unroll
      for (int k = 0; k < 4; ++k) v[k] += dpp<ROR8>(v[k]);
      const auto t0 = __builtin_amdgcn_permlane16_swap(__float_as_uint(v[0]), __float_as_uint(v[1]), false, false);
      const auto t1 = __builtin_amdgcn_permlane16_swap(__float_as_uint(v[2]), __float_as_uint(v[3]), false, false);
      const float a0 = __uint_as_float(t0[0]) + __uint_as_float(t0[1]), a1 = __uint_as_float(t1[0]) + __uint_as_float(t1[1]);
      const auto t2 = __builtin_amdgcn_permlane32_swap(__float_as_uint(a0), __float_as_uint(a1), false, false);
      if (jb == 0) ldf[L_P1 + wv * 32 + jj * 4 + r] = __uint_as_float(t2[0]) + __uint_as_float(t2[1]);
    } else {
      float v0 = __uint_as_float(g0[0]) + __uint_as_float(g1[0]), v1 = __uint_as_float(g0[2]) + __uint_as_float(g1[2]);
      v0 += dpp<ROR8>(v0); v1 += dpp<ROR8>(v1);
      const auto t0 = __builtin_amdgcn_permlane16_swap(__float_as_uint(v0), __float_as_uint(v1), false, false);
      const float a = x32sum(__uint_as_float(t0[0]) + __uint_as_float(t0[1]));
      if (jb == 0 && lane < 32) ldf[L_P1 + wv * 16 + jj * 2 + r] = a;
    }
    BAR();
    if constexpr (ROLE == R_HG) {
      float s;
      if (PFMODE == 2) { const float* fp = ldf + L_P1 + (lane >> 5) * 256 + (lane & 31);
        s = ((fp[0] + fp[32]) + (fp[64] + fp[96])) + ((fp[128] + fp[160]) + (fp[192] + fp[224])); s = x32sum(s); }
      else { const float* fp = ldf + L_P1 + r * 64 + c; s = x32sum(x16sum((fp[0] + fp[16]) + (fp[32] + fp[48]))); }
      if (lane < (PFMODE == 2 ? 32 : 16)) cx.out[oidx] = (u16)pkbf(bf2f(xres) + s, 0.f);
      if (w == 0 && lane == 0) __builtin_amdgcn_raw_buffer_store_b32(T, rs, WS_E, 0, 16);
      STAMP(14);
    }
  }
  STAMP_DUMP();
}

__global__ void __launch_bounds__(1024, 4) fused_decode(const u16* __restrict__ X, const u16* __restrict__ Kc, const u16* __restrict__ Vc,
                                                        const u16* __restrict__ Wq, const u16* __restrict__ Wo, const u16* __restrict__ Wg,
                                                        const u16* __restrict__ Wu, const u16* __restrict__ Wd, u16* __restrict__ out, char* ws) {
  __shared__ __attribute__((aligned(16))) u32 lds[L_TOTAL];
  float* ldf = reinterpret_cast<float*>(lds);
  const int tid = threadIdx.x, lane = tid & 63, wv = __builtin_amdgcn_readfirstlane(tid >> 6);
  const int w = blockIdx.x, xc = w & 7, idx = w >> 3, kvh = 2 * xc + (idx >> 4), jg = idx & 15;
  const int r = lane >> 4, c = lane & 15, jb = (lane >> 3) & 1, jj = lane & 7;
  asm volatile("" :: "s"(X), "s"(Kc), "s"(Vc), "s"(Wq), "s"(Wo), "s"(Wg), "s"(Wu), "s"(Wd), "s"(out), "s"(ws));
  STAMP_INIT();
  STAMP(0);
  u32 xr[5];
  if (wv < 2) {
    const u32* xp = reinterpret_cast<const u32*>(X) + wv * 288 + lane;
#pragma unroll
    for (int t = 0; t < 4; ++t) xr[t] = xp[64 * t];
    xr[4] = xp[lane < 32 ? 256 : 0];
  }
  const int s0 = (117 * idx) >> 1, cnt = 58 + (idx & 1), bA = s0 / 144, nA = min(cnt, 144 * (bA + 1) - s0), wA = (nA + 3) >> 2;
  const int beta = wv < wA ? bA : bA + 1, st0 = wv < wA ? s0 - 144 * bA + 4 * wv : 4 * (wv - wA);
  const int ns = wv < wA ? nA - 4 * wv : cnt - nA - 4 * (wv - wA);
  u32x4 a[4];
  const u32 str = beta < 4 ? 8192 : 9216;
  const int ub = beta - 4 + UPROT;
  gchar* p1 = beta < 4 ? (gchar*)Wq + 1024 * xc + 256 * beta + 16 * c
                       : (gchar*)(c < 8 ? Wg : Wu) + 1152 * xc + 128 * (c < 8 ? beta - 4 : (ub >= 9 ? ub - 9 : ub)) + 16 * (c & 7);
  p1 += (u32)(4 * st0 + r) * str;
#define L1LOAD(i) a[i] = ldnt(i < ns ? p1 + i * 4 * str : (gchar*)X)
  L1LOAD(0); L1LOAD(1);
  if (wv == 15 && lane < 8) lds[L_CNT + lane] = 0;
  BAR();
  const size_t kvo = ((size_t)(((jg >> 3) * 16 + kvh) * SK + (jg & 7) * 512 + wv * 32)) * 256 + lane * 16;
  gchar* kp = (gchar*)Kc + kvo;
  const u32 T = *reinterpret_cast<const __attribute__((address_space(4))) u32*>((uintptr_t)ws + WS_E) + 1u;
  const u32 mword = (jj & 1) ? 0xFFFF0000u : 0x0000FFFFu;
  const u32x4 mk = {(jj >> 1) == 0 ? mword : 0u, (jj >> 1) == 1 ? mword : 0u, (jj >> 1) == 2 ? mword : 0u, (jj >> 1) == 3 ? mword : 0u};
  if (wv >= 2) VMW(1);
  L1LOAD(2);
  CLOBBER();
  if (wv < 2) {
    float v[10], s = 0.f;
#pragma unroll
    for (int t = 0; t < 5; ++t) { v[2 * t] = bf2f(xr[t] & 0xffffu); v[2 * t + 1] = __uint_as_float(xr[t] & 0xffff0000u); }
#pragma unroll
    for (int t = 0; t < 8; ++t) s += v[t];
    if (lane < 32) s += v[8] + v[9];
    const float mean = wavesum(s) * (1.f / DM);
    float q = 0.f;
#pragma unroll
    for (int t = 0; t < 8; ++t) { const float d = v[t] - mean; q = fmaf(d, d, q); }
    if (lane < 32) { const float d = v[8] - mean, e = v[9] - mean; q = fmaf(d, d, fmaf(e, e, q)); }
    const float mb = rbf(mean), den = rbf(__builtin_sqrtf(rbf(rbf(wavesum(q) * (1.f / DM)) + 1e-5f)));
    u32* xd = lds + L_XN + wv * 580 + 2 * lane;
#pragma unroll
    for (int t = 0; t < 5; ++t) {
      const float y0 = rbf(v[2 * t] - mb) / den, y1 = rbf(v[2 * t + 1] - mb) / den;
      uint2 o = {pkbf(y0, y0), pkbf(y1, y1)};
      if (t < 4 || lane < 32) *reinterpret_cast<uint2*>(xd + 128 * t) = o;
    }
    arrive(lds + L_CNT + C_X, lane);
    STAMP(1);
  }
  VMW(1); L1LOAD(3);
  VMW(1);
  const u32x4 k0 = ldnt(kp);
  VMW(1);
  const u32x4 k1 = ldnt(kp + 1024);
  CLOBBER();
  await(lds + L_CNT + C_X, 2);
  {
    f32x4 acc = {0.f, 0.f, 0.f, 0.f};
    const u32* xb = lds + L_XN + jb * 580 + 4 * st0 + r;
#pragma unroll
    for (int i = 0; i < 4; ++i) { const u32 xv = xb[4 * i]; acc = rowmma(a[i], i < ns ? xv : 0u, mk, acc); }
    *reinterpret_cast<f32x4*>(ldf + L_P1 + wv * 256 + lane * 4) = acc;
    arrive(lds + L_CNT + C_P1, lane);
  }
  STAMP(2);
  Ctx cx;
  cx.X = X; cx.Wo = Wo; cx.Wd = Wd; cx.out = out; cx.ws = ws; cx.kp = kp; cx.vp = (gchar*)Vc + kvo; cx.lds = lds;
  cx.rs = __builtin_amdgcn_make_buffer_rsrc((void*)ws, 0, WS_END, 0x00020000); cx.mk = mk; cx.T = T;
  cx.lane = lane; cx.wv = wv; cx.w = w; cx.bA = bA; cx.wA = wA; cx.hasB = cnt > nA;
  if (wv < 4) rest<R_RED>(cx, k0, k1);
  else if (wv < 6) rest<R_QG>(cx, k0, k1);
  else if (wv == 6) rest<R_HG>(cx, k0, k1);
  else if (wv == 7) rest<R_ML>(cx, k0, k1);
  else if (wv < 11) rest<R_PLAIN>(cx, k0, k1);
  else if (wv < 15) rest<R_L2>(cx, k0, k1);
  else rest<R_L2H>(cx, k0, k1);
}

extern "C" void kernel_launch(void* const* d_in, const int* in_sizes, int n_in, void* d_out, int out_size, void* d_ws, size_t ws_size,
                              hipStream_t stream) {
  fused_decode<<<256, 1024, 0, stream>>>((const u16*)d_in[0], (const u16*)d_in[1], (const u16*)d_in[2], (const u16*)d_in[3], (const u16*)d_in[4],
                                         (const u16*)d_in[6], (const u16*)d_in[7], (const u16*)d_in[8], (u16*)d_out, (char*)d_ws);
}
```

```cpp
#include <hip/hip_runtime.h>
#include <hip/hip_bf16.h>
#include <stdint.h>

typedef unsigned short u16;
typedef unsigned int u32;
typedef unsigned long long u64;
using u32x4 = __attribute__((ext_vector_type(4))) unsigned;
using u32x3 = __attribute__((ext_vector_type(3))) unsigned;
using f32x4 = __attribute__((ext_vector_type(4))) float;
using f32x2 = __attribute__((ext_vector_type(2))) float;
using bf16x8 = __attribute__((ext_vector_type(8))) __bf16;
using bf16x2 = __attribute__((ext_vector_type(2))) __bf16;
typedef __attribute__((address_space(1))) const u32x4 gvec;
typedef __attribute__((address_space(1))) const char gchar;

constexpr int DM = 576, SK = 4096, FF = 4608;
constexpr u32 WS_E = 0;
constexpr u32 WS_PO = 65536;
constexpr u32 WS_P1 = WS_PO + 524288;
constexpr u32 WS_PF = WS_P1 + 8 * 13 * 4 * 1024;
constexpr u32 WS_END = WS_PF + 256 * 4608;
constexpr u32 SPIN_CAP = 1u << 20;

constexpr int L_XN = 0;
constexpr int L_CNT = 1160;
constexpr int L_Q = 1168;
constexpr int L_H = 1296;
constexpr int L_O = 1360;
constexpr int L_M = 1392;
constexpr int L_L = 1424;
constexpr int L_P1 = 1600;
constexpr int L_REC = L_P1 + 4096;
constexpr int L_ST = L_REC + 4096;
enum { C_P1 = 0, C_Q = 1, C_M = 2, C_X = 3 };
constexpr int L_TOTAL = L_ST;
#define STAMP(id)
#define STAMP_INIT()
#define STAMP_DUMP()
#define ODELAY 24
#define FDELAY 14
#define FASTDIV 1
constexpr int UPROT = 4;
constexpr u32 OOB = 0xFFFFFF00u;

__device__ __forceinline__ float bf2f(u32 x) { return __uint_as_float(x << 16); }
__device__ __forceinline__ float rbf(float x) { return (float)(__bf16)x; }
__device__ __forceinline__ u32 pkbf(float lo, float hi) { bf16x2 v = {(__bf16)lo, (__bf16)hi}; return __builtin_bit_cast(u32, v); }
__device__ __forceinline__ u32x4 ldnt(gchar* p) { return __builtin_nontemporal_load((gvec*)p); }
__device__ __forceinline__ float dot2(u32 a, u32 b, float c) {
  return __builtin_amdgcn_fdot2_f32_bf16(__builtin_bit_cast(bf16x2, a), __builtin_bit_cast(bf16x2, b), c, false);
}
template <int CTRL> __device__ __forceinline__ float dpp(float x) {
  return __builtin_bit_cast(float, __builtin_amdgcn_mov_dpp(__builtin_bit_cast(int, x), CTRL, 0xf, 0xf, true));
}
constexpr int XOR1 = 0xB1, XOR2 = 0x4E, XOR7 = 0x141, ROR8 = 0x128, ROR4 = 0x124;
__device__ __forceinline__ float x16sum(float x) {
  auto s = __builtin_amdgcn_permlane16_swap(__float_as_uint(x), __float_as_uint(x), false, false);
  return __uint_as_float(s[0]) + __uint_as_float(s[1]);
}
__device__ __forceinline__ float x32sum(float x) {
  auto s = __builtin_amdgcn_permlane32_swap(__float_as_uint(x), __float_as_uint(x), false, false);
  return __uint_as_float(s[0]) + __uint_as_float(s[1]);
}
__device__ __forceinline__ float x16max(float x) {
  auto s = __builtin_amdgcn_permlane16_swap(__float_as_uint(x), __float_as_uint(x), false, false);
  return fmaxf(__uint_as_float(s[0]), __uint_as_float(s[1]));
}
__device__ __forceinline__ float x32max(float x) {
  auto s = __builtin_amdgcn_permlane32_swap(__float_as_uint(x), __float_as_uint(x), false, false);
  return fmaxf(__uint_as_float(s[0]), __uint_as_float(s[1]));
}
__device__ __forceinline__ float wavesum(float s) {
  s += dpp<XOR1>(s); s += dpp<XOR2>(s); s += dpp<XOR7>(s); s += dpp<ROR8>(s); return x32sum(x16sum(s));
}
__device__ __forceinline__ float fdiv(float a, float b, float rb) { const float q = a * rb; return fmaf(fmaf(-q, b, a), rb, q); }
__device__ __forceinline__ f32x4 rowmma(u32x4 a, u32 bb, u32x4 mk, f32x4 acc) {
  u32x4 b = {bb & mk[0], bb & mk[1], bb & mk[2], bb & mk[3]};
  return __builtin_amdgcn_mfma_f32_16x16x32_bf16(__builtin_bit_cast(bf16x8, a), __builtin_bit_cast(bf16x8, b), acc, 0, 0, 0);
}
#define CLOBBER() asm volatile("" ::: "memory")
#define BAR() do { asm volatile("s_waitcnt lgkmcnt(0)" ::: "memory"); __builtin_amdgcn_s_barrier(); asm volatile("" ::: "memory"); } while (0)
#define VMW_(n) asm volatile("s_waitcnt vmcnt(" #n ")" ::: "memory")
#define VMW(n) VMW_(n)
#define RETRY(LOADS, OK, SL) do { if (!__all(OK)) { _Pragma("unroll 1") for (u32 spins = 0; spins < SPIN_CAP; ++spins) { \
      __builtin_amdgcn_s_sleep(SL); CLOBBER(); if (!(OK)) { LOADS; } if (__all(OK)) break; } } } while (0)
#define RETRY_ALL(LOADS, OK, SL) do { if (!__all(OK)) { _Pragma("unroll 1") for (u32 spins = 0; spins < SPIN_CAP; ++spins) { \
      __builtin_amdgcn_s_sleep(SL); CLOBBER(); LOADS; if (__all(OK)) break; } } } while (0)
__device__ __forceinline__ u32x4 gload(__amdgpu_buffer_rsrc_t r, u32 off) { return __builtin_amdgcn_raw_buffer_load_b128(r, off, 0, 16); }
__device__ __forceinline__ void qstore(__amdgpu_buffer_rsrc_t r, u32 off, f32x4 v, u32 H) {
  const u32x4 d = {__builtin_amdgcn_perm(__float_as_uint(v[0]), H, 0x07060500u), __builtin_amdgcn_perm(__float_as_uint(v[1]), H, 0x07060501u),
                   __builtin_amdgcn_perm(__float_as_uint(v[2]), H, 0x07060502u), __builtin_amdgcn_perm(__float_as_uint(v[3]), H, 0x07060503u)};
  __builtin_amdgcn_raw_buffer_store_b128(d, r, off, 0, 16);
}
__device__ __forceinline__ bool qok(u32x4 d, u32 H) { return (__builtin_amdgcn_perm(d[1], d[0], 0x0c0c0400u) | __builtin_amdgcn_perm(d[3], d[2], 0x04000c0cu)) == H; }
__device__ __forceinline__ f32x4 qval(u32x4 d) {
  return f32x4{__uint_as_float(d[0] & 0xFFFFFF00u), __uint_as_float(d[1] & 0xFFFFFF00u), __uint_as_float(d[2] & 0xFFFFFF00u), __uint_as_float(d[3] & 0xFFFFFF00u)};
}

#define TB 6
constexpr u32 TM = (1u << TB) - 1;
__device__ __forceinline__ u32x4 ftag(u32 T) { const u32 b = T ^ (T >> (2 * TB)); return u32x4{T & TM, (T >> TB) & TM, b & TM, (b >> TB) & TM}; }
__device__ __forceinline__ void fstore(__amdgpu_buffer_rsrc_t r, u32 off, f32x4 v, u32x4 n) {
  u32x4 d;
#pragma unroll
  for (int k = 0; k < 4; ++k) { const u32 u = __float_as_uint(v[k]); d[k] = (((u & 0x7F800000u) == 0x7F800000u ? u : u + (TM + 1) / 2) & ~TM) | n[k]; }
  __builtin_amdgcn_raw_buffer_store_b128(d, r, off, 0, 16);
}
__device__ __forceinline__ bool fok(u32x4 d, u32x4 n) { return (((d[0] ^ n[0]) | (d[1] ^ n[1]) | (d[2] ^ n[2]) | (d[3] ^ n[3])) & TM) == 0; }
__device__ __forceinline__ f32x4 fval(u32x4 d) {
  return f32x4{__uint_as_float(d[0] & ~TM), __uint_as_float(d[1] & ~TM), __uint_as_float(d[2] & ~TM), __uint_as_float(d[3] & ~TM)};
}

__device__ __forceinline__ void arrive(u32* p, int lane) {
  CLOBBER();
  if (lane == 0) __hip_atomic_fetch_add(p, 1u, __ATOMIC_RELAXED, __HIP_MEMORY_SCOPE_WORKGROUP);
  CLOBBER();
}
__device__ __forceinline__ void await(u32* p, u32 n) {
  _Pragma("unroll 1") for (u32 spins = 0; spins < SPIN_CAP; ++spins) {
    if (__builtin_amdgcn_readfirstlane(__hip_atomic_load(p, __ATOMIC_RELAXED, __HIP_MEMORY_SCOPE_WORKGROUP)) >= n) break;
    __builtin_amdgcn_s_sleep(1);
  }
  CLOBBER();
}
__device__ __forceinline__ u32x4 await_o(const char* p) {
  u32x4 v;
  _Pragma("unroll 1") for (u32 spins = 0; spins < SPIN_CAP; ++spins) {
    CLOBBER();
    v = *reinterpret_cast<const u32x4*>(p);
    if (__all(v[0] != 0xFFFFFFFFu && v[3] != 0xFFFFFFFFu)) break;
  }
  return v;
}
#define SB() __builtin_amdgcn_sched_barrier(0)

struct Ctx {
  const u16* X; const u16* Wo; const u16* Wd; u16* out; char* ws; gchar* kp; gchar* vp; u32* lds;
  __amdgpu_buffer_rsrc_t rs; u32x4 mk; u32 T;
  int lane, wv, w, bA, wA, hasB;
};
enum { R_RED, R_QG, R_HG, R_OG, R_PLAIN, R_L2, R_L2H };

template <int ROLE>
__device__ __forceinline__ void rest(const Ctx& cx, u32x4 k0, u32x4 k1) {
  u32* lds = cx.lds; float* ldf = reinterpret_cast<float*>(lds);
  const int lane = cx.lane, wv = cx.wv, w = cx.w, xc = w & 7, idx = w >> 3, kvh = 2 * xc + (idx >> 4), jg = idx & 15;
  const int r = lane >> 4, c = lane & 15, jb = (lane >> 3) & 1, jj = lane & 7;
  const int fs = 18 * idx + ((idx & 1) << 1), nf = (idx & 1) ? 16 : 20;
  const auto rs = cx.rs; const u32x4 mk = cx.mk; char* ws = cx.ws;
  const u32 T = cx.T, H = T ^ (T << 16);
  const u32x4 N = ftag(T);
  gchar* kp = cx.kp; gchar* vp = cx.vp;
  u32x4 kk[8], vv[8];
  kk[0] = k0; kk[1] = k1;
  if constexpr (ROLE == R_RED) {
    kk[2] = ldnt(kp + 2048); kk[3] = ldnt(kp + 3072);
    const int sgm = wv >> 1, bt = wv & 1, beta = cx.bA + sgm;
    const u32 off = WS_P1 + ((xc * 13 + beta) * 4 + idx - (288 * beta + 1) / 117) * 1024 + bt * 512 + lane * 16;
    const u32 sel = (sgm ? (0xFFFFu << cx.wA) : ~(0xFFFFu << cx.wA)) >> ((lane >> 5) * 8);
    const f32x4* rp = reinterpret_cast<const f32x4*>(ldf + L_P1) + (lane >> 5) * 512 + ((lane >> 3) & 3) * 16 + bt * 8 + jj;
    await(lds + L_CNT + C_P1, 16);
    f32x4 s = {0.f, 0.f, 0.f, 0.f};
#pragma unroll
    for (int k = 0; k < 8; ++k) s += (float)((sel >> k) & 1u) * rp[k * 64];
#pragma unroll
    for (int k = 0; k < 4; ++k) s[k] = x32sum(s[k]);
    fstore(rs, lane < 32 && (sgm == 0 || cx.hasB) ? off : OOB, s, N);
    STAMP(3);
    VMW(2); kk[4] = ldnt(kp + 4096);
    VMW(2); kk[5] = ldnt(kp + 5120);
    VMW(2); kk[6] = ldnt(kp + 6144);
    VMW(1); kk[7] = ldnt(kp + 7168);
  } else {
    VMW(1); kk[2] = ldnt(kp + 2048);
    VMW(1); kk[3] = ldnt(kp + 3072);
    VMW(1); kk[4] = ldnt(kp + 4096);
    VMW(1); kk[5] = ldnt(kp + 5120);
    if constexpr (ROLE == R_QG) {
      const int beta = 2 * (idx >> 4) + wv - 4;
      const bool has2 = lane < 32 || (288 * beta + 287) / 117 - (288 * beta + 1) / 117 == 3;
      const u32 off = WS_P1 + ((xc * 13 + beta) * 4 + (lane >> 5)) * 1024 + (jg >> 3) * 512 + (lane & 31) * 16, off2 = has2 ? off + 2048 : OOB;
      u32x4 g0, g1;
      VMW(1);
#define LOADS_ { g0 = gload(rs, off); g1 = gload(rs, off2); }
      LOADS_
      kk[6] = ldnt(kp + 6144); kk[7] = ldnt(kp + 7168);
      CLOBBER();
      RETRY_ALL(LOADS_, fok(g0, N) && (fok(g1, N) || !has2), 8);
#undef LOADS_
      const f32x4 v = fval(g0) + fval(g1);
      const auto t0 = __builtin_amdgcn_permlane32_swap(__float_as_uint(v[0]), __float_as_uint(v[2]), false, false);
      const auto t1 = __builtin_amdgcn_permlane32_swap(__float_as_uint(v[1]), __float_as_uint(v[3]), false, false);
      const u32 pk = pkbf(__uint_as_float(t0[0]) + __uint_as_float(t0[1]), __uint_as_float(t1[0]) + __uint_as_float(t1[1]));
      u16* qd = reinterpret_cast<u16*>(lds + L_Q) + (wv - 4) * 128 + 32 * ((lane >> 3) & 3) + 16 * (lane >> 5) + jj;
      qd[0] = (u16)pk; qd[8] = (u16)(pk >> 16);
      arrive(lds + L_CNT + C_Q, lane);
      STAMP(3);
    } else {
      VMW(1); kk[6] = ldnt(kp + 6144);
      VMW(1); kk[7] = ldnt(kp + 7168);
    }
  }
  VMW(1); vv[0] = ldnt(vp);
  u32 touch = 0;
  if constexpr (ROLE == R_OG) {
    touch = __builtin_amdgcn_raw_buffer_load_b32(rs, lane < 32 ? WS_PO + (kvh * 16 + jg) * 2048 + lane * 64 : OOB, 0, 16);
    VMW(2);
  } else VMW(1);
  vv[1] = ldnt(vp + 1024);
  VMW(1); vv[2] = ldnt(vp + 2048);
  CLOBBER();
  asm volatile("" :: "v"(touch));
  await(lds + L_CNT + C_Q, 2);
  STAMP(4);
  float x[16];
  {
    const u32x4 q0 = *reinterpret_cast<const u32x4*>(lds + L_Q + 4 * c), q1 = *reinterpret_cast<const u32x4*>(lds + L_Q + 64 + 4 * c);
#pragma unroll
    for (int i = 0; i < 8; ++i) {
      if (i == 5) { SB(); VMW(1); vv[3] = ldnt(vp + 3072); SB(); }
      float d0 = 0.f, d1 = 0.f;
#pragma unroll
      for (int k = 0; k < 4; ++k) { d0 = dot2(kk[i][k], q0[k], d0); d1 = dot2(kk[i][k], q1[k], d1); }
      x[2 * i] = d0; x[2 * i + 1] = d1;
    }
  }
#define TR_STEP(HALF, CTRL) _Pragma("unroll") for (int h = 0; h < (HALF); ++h) { \
    const float t1_ = x[h] + dpp<CTRL>(x[h]), t2_ = x[h + (HALF)] + dpp<CTRL>(x[h + (HALF)]); x[h] = (c & (HALF)) ? t2_ : t1_; }
  TR_STEP(8, ROR8)
  u32x4 g[4], u[4]; u32 go = 0, uo = 0, go3 = 0, uo3 = 0;
#define HLOADS_ { _Pragma("unroll") for (int k = 0; k < 3; ++k) { g[k] = gload(rs, go + k * 1024); u[k] = gload(rs, uo + k * 1024); } g[3] = gload(rs, go3); u[3] = gload(rs, uo3); }
  const int fi = min(lane & 31, nf - 1), ff = fs + fi;
  if constexpr (ROLE == R_HG) {
    const int cb = ff >> 6, bg = 4 + cb, bu = 4 + (cb + 9 - UPROT) % 9;
    const u32 ho = WS_P1 + xc * 13 * 4096 + (lane >> 5) * 512 + ((ff >> 5) & 1) * 128 + (ff & 7) * 16;
    go = ho + bg * 4096; uo = ho + bu * 4096 + 256;
    go3 = (288 * bg + 287) / 117 - (288 * bg + 1) / 117 == 3 ? go + 3072 : OOB;
    uo3 = (288 * bu + 287) / 117 - (288 * bu + 1) / 117 == 3 ? uo + 3072 : OOB;
    SB(); VMW(1);
    HLOADS_
    vv[4] = ldnt(vp + 4096);
    SB();
  } else { SB(); VMW(1); vv[4] = ldnt(vp + 4096); SB(); }
  TR_STEP(4, XOR7) TR_STEP(2, XOR2) TR_STEP(1, XOR1)
#undef TR_STEP
  u32 bb[8];
  {
    const float sc = rbf(rbf(x[0]) * 0.08838834764831845f);
    float m = fmaxf(sc, dpp<XOR2>(sc)); m = fmaxf(m, dpp<ROR4>(m)); m = fmaxf(m, dpp<ROR8>(m)); m = x32max(x16max(m));
    const float p = __builtin_amdgcn_exp2f((sc - m) * 1.4426950408889634f);
    float l = p + dpp<XOR2>(p); l += dpp<ROR4>(l); l += dpp<ROR8>(l); l = x32sum(x16sum(l));
    if (lane < 2) { ldf[L_M + lane * 16 + wv] = m; ldf[L_L + lane * 16 + wv] = l; }
    arrive(lds + L_CNT + C_M, lane);
    STAMP(5);
    if constexpr (ROLE == R_HG) { vv[5] = ldnt(vp + 5120); CLOBBER(); } else { SB(); VMW(1); vv[5] = ldnt(vp + 5120); SB(); }
    const int pb = (int)pkbf(p, p);
#define PBC(I) { int t_ = __builtin_amdgcn_update_dpp(0, pb, 0x150 + 2 * (I), 0xf, 0x3, false); bb[I] = (u32)__builtin_amdgcn_update_dpp(t_, pb, 0x151 + 2 * (I), 0xf, 0xc, false); }
    PBC(0) PBC(1) PBC(2) PBC(3) PBC(4) PBC(5) PBC(6) PBC(7)
#undef PBC
  }
  f32x4 acc = {0.f, 0.f, 0.f, 0.f};
#pragma unroll
  for (int i = 0; i < 4; ++i) acc = rowmma(vv[i], bb[i], mk, acc);
  SB();
  if constexpr (ROLE == R_HG) {
    RETRY_ALL(HLOADS_, fok(g[0], N) && fok(g[1], N) && fok(g[2], N) && (fok(g[3], N) || go3 == OOB) &&
                       fok(u[0], N) && fok(u[1], N) && fok(u[2], N) && (fok(u[3], N) || uo3 == OOB), 8);
    vv[6] = ldnt(vp + 6144);
    const f32x4 gs = (fval(g[0]) + fval(g[1])) + (fval(g[2]) + fval(g[3])), us = (fval(u[0]) + fval(u[1])) + (fval(u[2]) + fval(u[3]));
    float g0 = gs[0], g1 = gs[1], g2 = gs[2], g3 = gs[3], u0 = us[0], u1 = us[1], u2 = us[2], u3 = us[3];
    asm volatile("" : "+v"(g0), "+v"(g1), "+v"(g2), "+v"(g3), "+v"(u0), "+v"(u1), "+v"(u2), "+v"(u3));
    const bool b3 = (ff & 8) != 0, b4 = (ff & 16) != 0;
    const float h = rbf(b4 ? (b3 ? g3 : g2) : (b3 ? g1 : g0)) * rbf(b4 ? (b3 ? u3 : u2) : (b3 ? u1 : u0));
    if ((lane & 31) < nf) lds[L_H + ((lane >> 5) * 4 + (fi & 3)) * 8 + (fi >> 2)] = pkbf(h, h);
    STAMP(3);
    VMW(2); vv[7] = ldnt(vp + 7168); CLOBBER();
    acc = rowmma(vv[4], bb[4], mk, acc); acc = rowmma(vv[5], bb[5], mk, acc);
  } else {
#pragma unroll
    for (int i = 4; i < 6; ++i) { VMW(1); vv[i + 2] = ldnt(vp + (i + 2) * 1024); CLOBBER(); acc = rowmma(vv[i], bb[i], mk, acc); SB(); }
  }
#undef HLOADS_
  const u32 pfo = WS_PF + (kvh * 16 + jg) * 4608;
  const int gam = 8 * w + ((lane >> 2) & 7), gl = gam < 256 ? (gam & 63) : (gam & 31);
  const int oidx = ((gl >> 3) & 1) * DM + (gam < 256 ? 128 * (gam >> 6) : 512) + 32 * (gl >> 4) + (gl & 7) + 8 * (lane & 3);
  u32 xres = 0;
#define PV_TAIL() do { acc = rowmma(vv[6], bb[6], mk, acc); acc = rowmma(vv[7], bb[7], mk, acc); \
    *reinterpret_cast<f32x4*>(ldf + L_REC + wv * 256 + lane * 4) = acc; STAMP(6); BAR(); } while (0)
  if constexpr (ROLE == R_L2) {
    gchar* dp = (gchar*)cx.Wd + (size_t)(576 * xc + fs + r) * 1152 + 256 * (wv - 11) + 16 * c;
    gchar* op = (gchar*)cx.Wo + (size_t)(256 * kvh + 16 * jg + r) * 1152 + 256 * (wv - 11) + 16 * c;
    u32x4 ah[5], ao[4];
    VMW(1);
#pragma unroll
    for (int t = 0; t < 4; ++t) ah[t] = ldnt(dp + t * 4608);
    ah[4] = ldnt(nf == 20 ? dp + 4 * 4608 : (gchar*)cx.X);
    CLOBBER();
    PV_TAIL();
#pragma unroll
    for (int t = 0; t < 4; ++t) ao[t] = ldnt(op + t * 4608);
    CLOBBER();
    STAMP(8);
    const u32x4 hb = *reinterpret_cast<const u32x4*>(lds + L_H + (jb * 4 + r) * 8);
    const u32 hb4 = nf == 20 ? lds[L_H + (jb * 4 + r) * 8 + 4] : 0u;
    f32x4 accA = {0.f, 0.f, 0.f, 0.f};
#pragma unroll
    for (int t = 0; t < 4; ++t) accA = rowmma(ah[t], hb[t], mk, accA);
    accA = rowmma(ah[4], hb4, mk, accA);
    u32 off = pfo + (wv - 11) * 1024 + lane * 16, oa = (L_O + (jb * 4 + r) * 4) * 4;
    asm volatile("s_waitcnt vmcnt(0)" : "+v"(accA), "+v"(off), "+v"(oa) :: "memory");
    STAMP(10);
    const u32x4 ob = await_o(reinterpret_cast<const char*>(lds) + oa);
#pragma unroll
    for (int t = 0; t < 4; ++t) accA = rowmma(ao[t], ob[t], mk, accA);
    qstore(rs, off, accA, H);
    STAMP(9);
  } else if constexpr (ROLE == R_L2H) {
    gchar* dp = (gchar*)cx.Wd + (size_t)(576 * xc + fs + r) * 1152 + 1024 + 16 * jj;
    gchar* op = (gchar*)cx.Wo + (size_t)(256 * kvh + 16 * jg + r + 4 * jb) * 1152 + 1024 + 16 * jj;
    u32x4 ah[3], ao[2];
    VMW(1);
    ah[0] = ldnt(dp + jb * 4608); ah[1] = ldnt(dp + jb * 4608 + 9216);
    ah[2] = ldnt(nf == 20 ? dp + 4 * 4608 : (gchar*)cx.X);
    CLOBBER();
    PV_TAIL();
    ao[0] = ldnt(op); ao[1] = ldnt(op + 9216);
    CLOBBER();
    STAMP(8);
    const u32x4 hb = *reinterpret_cast<const u32x4*>(lds + L_H + (jb * 4 + r) * 8);
    const u32 hb4 = nf == 20 ? lds[L_H + (jb * 4 + r) * 8 + 4] : 0u;
    f32x4 accA = {0.f, 0.f, 0.f, 0.f}, accB = {0.f, 0.f, 0.f, 0.f};
    accA = rowmma(ah[0], hb[0], mk, accA); accB = rowmma(ah[0], hb[1], mk, accB);
    accA = rowmma(ah[1], hb[2], mk, accA); accB = rowmma(ah[1], hb[3], mk, accB);
    accA = rowmma(ah[2], hb4, mk, accA);
    u32 off = lane < 32 ? pfo + 4096 + lane * 16 : OOB, oa = (L_O + (jb * 4 + r) * 4) * 4;
    asm volatile("s_waitcnt vmcnt(0)" : "+v"(accA), "+v"(accB), "+v"(off), "+v"(oa) :: "memory");
    STAMP(10);
    const u32x4 ob = await_o(reinterpret_cast<const char*>(lds) + oa);
    accA = rowmma(ao[0], ob[0], mk, accA); accB = rowmma(ao[0], ob[1], mk, accB);
    accA = rowmma(ao[1], ob[2], mk, accA); accB = rowmma(ao[1], ob[3], mk, accB);
#pragma unroll
    for (int k = 0; k < 4; ++k) {
      const auto t = __builtin_amdgcn_permlane32_swap(__float_as_uint(accB[k]), __float_as_uint(accB[k]), false, false);
      accA[k] += __uint_as_float(t[1]);
    }
    qstore(rs, off, accA, H);
    STAMP(9);
  } else if constexpr (ROLE == R_RED) {
    await(lds + L_CNT + C_M, 16);
    const f32x4 m4 = *reinterpret_cast<const f32x4*>(ldf + L_M + jb * 16 + 4 * r), l4 = *reinterpret_cast<const f32x4*>(ldf + L_L + jb * 16 + 4 * r);
    const float M = x32max(x16max(fmaxf(fmaxf(m4[0], m4[1]), fmaxf(m4[2], m4[3]))));
    f32x4 fw;
#pragma unroll
    for (int k = 0; k < 4; ++k) fw[k] = __builtin_amdgcn_exp2f((m4[k] - M) * 1.4426950408889634f);
    const u32 Lb = __float_as_uint(x32sum(x16sum((fw[0] * l4[0] + fw[1] * l4[1]) + (fw[2] * l4[2] + fw[3] * l4[3]))));
    u32 tg0 = jj < 4 ? T : (T << 16) | (Lb & 0xFFFFu), tg1 = (T << 16) | (jj < 4 ? __float_as_uint(M) >> 16 : Lb >> 16);
    u32 off = lane < 32 ? WS_PO + ((kvh * 16 + 8 * jb + 2 * wv + r) * 16 + jg) * 128 + jj * 16 : OOB, ra = (L_REC + r * 1024 + (wv * 16 + c) * 4) * 4;
    asm volatile("" : "+v"(fw), "+v"(tg0), "+v"(tg1), "+v"(off), "+v"(ra));
    PV_TAIL();
    const f32x4* rp = reinterpret_cast<const f32x4*>(reinterpret_cast<const char*>(lds) + ra);
    const f32x4 s = (fw[0] * rp[0] + fw[1] * rp[64]) + (fw[2] * rp[128] + fw[3] * rp[192]);
    const auto t0 = __builtin_amdgcn_permlane16_swap(__float_as_uint(s[0]), __float_as_uint(s[2]), false, false);
    const auto t1 = __builtin_amdgcn_permlane16_swap(__float_as_uint(s[1]), __float_as_uint(s[3]), false, false);
    const u32x4 gv = {__float_as_uint(x32sum(__uint_as_float(t0[0]) + __uint_as_float(t0[1]))), tg0, __float_as_uint(x32sum(__uint_as_float(t1[0]) + __uint_as_float(t1[1]))), tg1};
    __builtin_amdgcn_raw_buffer_store_b128(gv, rs, off, 0, 16);
    STAMP(7);
  } else if constexpr (ROLE == R_OG) {
    u32 oo = WS_PO + ((kvh * 16 + jg) * 16 + c) * 128 + r * 16, oa = (L_O + (jb * 4 + r) * 4) * 4;
    asm volatile("" : "+v"(oo), "+v"(oa));
    PV_TAIL();
    u32x4 p0, p1;
    __builtin_amdgcn_s_sleep(ODELAY);
    CLOBBER();
#define LOADS_ { p0 = gload(rs, oo); p1 = gload(rs, oo + 64); }
#define OK_ (p0[1] == T && ((p0[3] ^ (T << 16)) | (p1[1] ^ (T << 16)) | (p1[3] ^ (T << 16))) < 0x10000u)
    LOADS_
    RETRY(LOADS_, OK_, 0);
#undef LOADS_
#undef OK_
    STAMP(11);
    const float mm = __uint_as_float(p0[3] << 16), ll = __uint_as_float((p1[3] << 16) | (p1[1] & 0xFFFFu));
    float Mx = fmaxf(mm, dpp<XOR1>(mm)); Mx = fmaxf(Mx, dpp<XOR2>(Mx)); Mx = fmaxf(Mx, dpp<XOR7>(Mx));
    const float e = __builtin_amdgcn_exp2f((mm - Mx) * 1.4426950408889634f);
    float o[5] = {e * __uint_as_float(p0[0]), e * __uint_as_float(p1[0]), e * __uint_as_float(p0[2]), e * __uint_as_float(p1[2]), e * ll};
#pragma unroll
    for (int t = 0; t < 5; ++t) asm volatile("" : "+v"(o[t]));
#pragma unroll
    for (int t = 0; t < 5; ++t) { o[t] += dpp<XOR1>(o[t]); o[t] += dpp<XOR2>(o[t]); o[t] += dpp<XOR7>(o[t]); }
    const float inv = __builtin_amdgcn_rcpf(o[4]);
    u32x4 ob;
#pragma unroll
    for (int t = 0; t < 4; ++t) { const float y = o[t] * inv; ob[t] = min(pkbf(y, y), 0xFFFFFFFEu); }
    *reinterpret_cast<u32x4*>(reinterpret_cast<char*>(lds) + oa) = ob;
  } else {
    PV_TAIL();
    if constexpr (ROLE == R_HG) { if (w < 36) xres = cx.X[oidx]; }
  }
  if (w >= 36) { STAMP_DUMP(); return; }
  u32 foff = WS_PF + (wv * 16 + (lane >> 3)) * 4608 + w * 128 + jj * 16;
  asm volatile("" : "+v"(foff));
  BAR();
  STAMP(12);
  __builtin_amdgcn_s_sleep(FDELAY);
  {
    u32x4 g0 = gload(rs, foff), g1 = gload(rs, foff + 8 * 4608);
    if (!__all(qok(g0, H) && qok(g1, H))) {
      _Pragma("unroll 1") for (u32 spins = 0; spins < SPIN_CAP; ++spins) {
        CLOBBER();
        if (!qok(g0, H)) g0 = gload(rs, foff);
        if (!qok(g1, H)) g1 = gload(rs, foff + 8 * 4608);
        if (__all(qok(g0, H) && qok(g1, H))) break;
      }
    }
    STAMP(13);
    f32x4 v = qval(g0) + qval(g1);
#pragma unroll
    for (int k = 0; k < 4; ++k) v[k] += dpp<ROR8>(v[k]);
    const auto t0 = __builtin_amdgcn_permlane16_swap(__float_as_uint(v[0]), __float_as_uint(v[1]), false, false);
    const auto t1 = __builtin_amdgcn_permlane16_swap(__float_as_uint(v[2]), __float_as_uint(v[3]), false, false);
    const float a0 = __uint_as_float(t0[0]) + __uint_as_float(t0[1]), a1 = __uint_as_float(t1[0]) + __uint_as_float(t1[1]);
    const auto t2 = __builtin_amdgcn_permlane32_swap(__float_as_uint(a0), __float_as_uint(a1), false, false);
    if (jb == 0) ldf[L_P1 + wv * 32 + jj * 4 + r] = __uint_as_float(t2[0]) + __uint_as_float(t2[1]);
    BAR();
    if constexpr (ROLE == R_HG) {
      const float* fp = ldf + L_P1 + (lane >> 5) * 256 + (lane & 31);
      const float s = x32sum(((fp[0] + fp[32]) + (fp[64] + fp[96])) + ((fp[128] + fp[160]) + (fp[192] + fp[224])));
      if (lane < 32) cx.out[oidx] = (u16)pkbf(bf2f(xres) + s, 0.f);
      if (w == 0 && lane == 0) __builtin_amdgcn_raw_buffer_store_b32(T, rs, WS_E, 0, 16);
      STAMP(14);
    }
  }
  STAMP_DUMP();
}

__global__ void __launch_bounds__(1024, 4) fused_decode(const u16* __restrict__ X, const u16* __restrict__ Kc, const u16* __restrict__ Vc,
                                                        const u16* __restrict__ Wq, const u16* __restrict__ Wo, const u16* __restrict__ Wg,
                                                        const u16* __restrict__ Wu, const u16* __restrict__ Wd, u16* __restrict__ out, char* ws) {
  __shared__ __attribute__((aligned(16))) u32 lds[L_TOTAL];
  float* ldf = reinterpret_cast<float*>(lds);
  const int tid = threadIdx.x, lane = tid & 63, wv = __builtin_amdgcn_readfirstlane(tid >> 6);
  const int w = blockIdx.x, xc = w & 7, idx = w >> 3, kvh = 2 * xc + (idx >> 4), jg = idx & 15;
  const int r = lane >> 4, c = lane & 15, jb = (lane >> 3) & 1, jj = lane & 7;
  asm volatile("" :: "s"(X), "s"(Kc), "s"(Vc), "s"(Wq), "s"(Wo), "s"(Wg), "s"(Wu), "s"(Wd), "s"(out), "s"(ws));
  STAMP_INIT();
  STAMP(0);
  u32 xr[5];
  if (wv < 2) {
    const u32* xp = reinterpret_cast<const u32*>(X) + wv * 288 + lane;
#pragma unroll
    for (int t = 0; t < 4; ++t) xr[t] = xp[64 * t];
    xr[4] = xp[lane < 32 ? 256 : 0];
  }
  const int s0 = (117 * idx) >> 1, cnt = 58 + (idx & 1), bA = s0 / 144, nA = min(cnt, 144 * (bA + 1) - s0), wA = (nA + 3) >> 2;
  const int beta = wv < wA ? bA : bA + 1, st0 = wv < wA ? s0 - 144 * bA + 4 * wv : 4 * (wv - wA);
  const int ns = wv < wA ? nA - 4 * wv : cnt - nA - 4 * (wv - wA);
  u32x4 a[4];
  const u32 str = beta < 4 ? 8192 : 9216;
  const int ub = beta - 4 + UPROT;
  gchar* p1 = beta < 4 ? (gchar*)Wq + 1024 * xc + 256 * beta + 16 * c
                       : (gchar*)(c < 8 ? Wg : Wu) + 1152 * xc + 128 * (c < 8 ? beta - 4 : (ub >= 9 ? ub - 9 : ub)) + 16 * (c & 7);
  p1 += (u32)(4 * st0 + r) * str;
#define L1LOAD(i) a[i] = ldnt(i < ns ? p1 + i * 4 * str : (gchar*)X)
  L1LOAD(0); L1LOAD(1);
  if (wv == 15 && lane < 8) lds[L_CNT + lane] = 0;
  if (wv == 14 && lane < 32) lds[L_O + lane] = 0xFFFFFFFFu;
  BAR();
  const size_t kvo = ((size_t)(((jg >> 3) * 16 + kvh) * SK + (jg & 7) * 512 + wv * 32)) * 256 + lane * 16;
  gchar* kp = (gchar*)Kc + kvo;
  u32 T = *reinterpret_cast<const __attribute__((address_space(4))) u32*>((uintptr_t)ws + WS_E) + 1u;
  const u32 mword = (jj & 1) ? 0xFFFF0000u : 0x0000FFFFu;
  const u32x4 mk = {(jj >> 1) == 0 ? mword : 0u, (jj >> 1) == 1 ? mword : 0u, (jj >> 1) == 2 ? mword : 0u, (jj >> 1) == 3 ? mword : 0u};
  if (wv >= 2) VMW(1);
  L1LOAD(2);
  CLOBBER();
  if (wv < 2) {
    float v[10], s = 0.f;
#pragma unroll
    for (int t = 0; t < 5; ++t) { v[2 * t] = bf2f(xr[t] & 0xffffu); v[2 * t + 1] = __uint_as_float(xr[t] & 0xffff0000u); }
#pragma unroll
    for (int t = 0; t < 8; ++t) s += v[t];
    if (lane < 32) s += v[8] + v[9];
    const float mean = wavesum(s) * (1.f / DM);
    float q = 0.f;
#pragma unroll
    for (int t = 0; t < 8; ++t) { const float d = v[t] - mean; q = fmaf(d, d, q); }
    if (lane < 32) { const float d = v[8] - mean, e = v[9] - mean; q = fmaf(d, d, fmaf(e, e, q)); }
    const float mb = rbf(mean), den = rbf(__builtin_sqrtf(rbf(rbf(wavesum(q) * (1.f / DM)) + 1e-5f)));
    const float rden = __builtin_amdgcn_rcpf(den);
    u32* xd = lds + L_XN + wv * 580 + 2 * lane;
#pragma unroll
    for (int t = 0; t < 5; ++t) {
      const float d0 = rbf(v[2 * t] - mb), d1 = rbf(v[2 * t + 1] - mb);
      const float y0 = FASTDIV ? fdiv(d0, den, rden) : d0 / den, y1 = FASTDIV ? fdiv(d1, den, rden) : d1 / den;
      uint2 o = {pkbf(y0, y0), pkbf(y1, y1)};
      if (t < 4 || lane < 32) *reinterpret_cast<uint2*>(xd + 128 * t) = o;
    }
    arrive(lds + L_CNT + C_X, lane);
    STAMP(1);
  }
  VMW(1); L1LOAD(3);
  VMW(1);
  const u32x4 k0 = ldnt(kp);
  VMW(1);
  const u32x4 k1 = ldnt(kp + 1024);
  CLOBBER();
  await(lds + L_CNT + C_X, 2);
  {
    f32x4 acc = {0.f, 0.f, 0.f, 0.f};
    const u32* xb = lds + L_XN + jb * 580 + 4 * st0 + r;
#pragma unroll
    for (int i = 0; i < 4; ++i) { const u32 xv = xb[4 * i]; acc = rowmma(a[i], i < ns ? xv : 0u, mk, acc); }
    *reinterpret_cast<f32x4*>(ldf + L_P1 + wv * 256 + lane * 4) = acc;
    arrive(lds + L_CNT + C_P1, lane);
  }
  asm volatile("" : "+s"(T));
  STAMP(2);
  Ctx cx;
  cx.X = X; cx.Wo = Wo; cx.Wd = Wd; cx.out = out; cx.ws = ws; cx.kp = kp; cx.vp = (gchar*)Vc + kvo; cx.lds = lds;
  cx.rs = __builtin_amdgcn_make_buffer_rsrc((void*)ws, 0, WS_END, 0x00020000); cx.mk = mk; cx.T = T;
  cx.lane = lane; cx.wv = wv; cx.w = w; cx.bA = bA; cx.wA = wA; cx.hasB = cnt > nA;
  if (wv < 4) rest<R_RED>(cx, k0, k1);
  else if (wv < 6) rest<R_QG>(cx, k0, k1);
  else if (wv == 6) rest<R_HG>(cx, k0, k1);
  else if (wv == 7) rest<R_OG>(cx, k0, k1);
  else if (wv < 11) rest<R_PLAIN>(cx, k0, k1);
  else if (wv < 15) rest<R_L2>(cx, k0, k1);
  else rest<R_L2H>(cx, k0, k1);
}

extern "C" void kernel_launch(void* const* d_in, const int* in_sizes, int n_in, void* d_out, int out_size, void* d_ws, size_t ws_size,
                              hipStream_t stream) {
  fused_decode<<<256, 1024, 0, stream>>>((const u16*)d_in[0], (const u16*)d_in[1], (const u16*)d_in[2], (const u16*)d_in[3], (const u16*)d_in[4],
                                         (const u16*)d_in[6], (const u16*)d_in[7], (const u16*)d_in[8], (u16*)d_out, (char*)d_ws);
}
```
